```python
import torch

def _softmax(x, axis=-1):
    return torch.softmax(x, dim=axis)
def _gelu(x):
    return torch.nn.functional.gelu(x, approximate="tanh")
def _silu(x):
    return torch.nn.functional.silu(x)
def _sigmoid(x):
    return torch.sigmoid(x)
def _layernorm(x, axis=-1, eps=1e-5):
    m = x.mean(dim=axis, keepdim=True)
    v = x.var(dim=axis, keepdim=True, unbiased=False)
    return (x - m) / torch.sqrt(v + eps)
def _scatter_add(tgt, idx, src, axis=0):
    return tgt.clone().index_add_(axis, idx, src)
def _segment_sum(src, seg, n):
    out = torch.zeros((n,) + src.shape[1:], dtype=src.dtype)
    return out.index_add_(0, seg, src)
def _pad(x, pw):
    flat = tuple(v for pair in reversed(pw) for v in pair)
    return torch.nn.functional.pad(x, flat)

kernel_name = 'parallel_sum__grouped_kv_mqa__dense_activation_gelu__prompt'

def setup_inputs():

    _w = (lambda a: (lambda t: t.to(torch.bfloat16) if t.is_floating_point() else t)(torch.as_tensor(a)))
    def _A_setup():
        B, S, D, H = 1, 2048, 512, 32
        Dh = D // H
        return {
            "x": _w((lambda *s: torch.randn(*s, dtype=torch.bfloat16) * 0.05)(B, S, D)),
            "wq": _w((lambda *s: torch.randn(*s, dtype=torch.bfloat16) * 0.05)(D, H * Dh)),
            "wk": _w((lambda *s: torch.randn(*s, dtype=torch.bfloat16) * 0.05)(D, Dh)),
            "wv": _w((lambda *s: torch.randn(*s, dtype=torch.bfloat16) * 0.05)(D, Dh)),
            "wo": _w((lambda *s: torch.randn(*s, dtype=torch.bfloat16) * 0.05)(H * Dh, D)),
        }
    _A_in = _A_setup()

    def _B_setup():
        B, S, D, F = 1, 2048, 512, 1376
        return {
            "x": _w((lambda *s: torch.randn(*s, dtype=torch.bfloat16) * 0.05)(B, S, D)),
            "w1": _w((lambda *s: torch.randn(*s, dtype=torch.bfloat16) * 0.8)(D, F) * (D ** -0.5)),
            "b1": _w((lambda *s: torch.randn(*s, dtype=torch.bfloat16) * 12.8)(F) * 0.01),
            "w2": _w((lambda *s: torch.randn(*s, dtype=torch.bfloat16) * 0.8)(F, D) * (F ** -0.5)),
            "b2": _w((lambda *s: torch.randn(*s, dtype=torch.bfloat16) * 12.8)(D) * 0.01),
        }
    _B_in = _B_setup()

    return {
        "A_x": _A_in["x"],
        "A_wq": _A_in["wq"],
        "A_wk": _A_in["wk"],
        "A_wv": _A_in["wv"],
        "A_wo": _A_in["wo"],
        "B_x": _B_in["x"],
        "B_w1": _B_in["w1"],
        "B_b1": _B_in["b1"],
        "B_w2": _B_in["w2"],
        "B_b2": _B_in["b2"],
    }

def reference(A_x, A_wq, A_wk, A_wv, A_wo, B_x, B_w1, B_b1, B_w2, B_b2):
    def _A_ref(x, wq, wk, wv, wo):
        B, S, D = x.shape
        H = 32
        Dh = D // H
        q = (lambda x, *d: x.permute(*d))((x @ wq).reshape(B, S, H, Dh), 0, 2, 1, 3)
        k = x @ wk
        v = x @ wv
        scores = torch.einsum("bhqd,bkd->bhqk", q, k) * (Dh ** -0.5)
        p = _softmax(scores, axis=-1)
        o = torch.einsum("bhqk,bkd->bhqd", p, v)
        return (lambda x, *d: x.permute(*d))(o, 0, 2, 1, 3).reshape(B, S, H * Dh) @ wo

    def _B_ref(x, w1, b1, w2, b2):
        h = torch.einsum("bsd,df->bsf", x, w1) + b1
        h = _gelu(h)
        return torch.einsum("bsf,fd->bsd", h, w2) + b2

    x = A_x
    xn = _layernorm(x, axis=-1)
    return x + _A_ref(xn, A_wq, A_wk, A_wv, A_wo) + _B_ref(xn, B_w1, B_b1, B_w2, B_b2)
```

```cpp
#include <hip/hip_runtime.h>
#include <hip/hip_bf16.h>
#include <stdint.h>

typedef __attribute__((ext_vector_type(16))) float f32x16;
typedef __attribute__((ext_vector_type(8))) __bf16 bf16x8;
typedef __attribute__((ext_vector_type(2))) __bf16 bf16x2;
typedef __attribute__((ext_vector_type(4))) unsigned u32x4;
typedef __attribute__((ext_vector_type(2))) unsigned u32x2;
typedef __attribute__((ext_vector_type(4))) float f32x4;
typedef __attribute__((ext_vector_type(2))) float f32x2;
typedef short v4i16 __attribute__((ext_vector_type(4)));
typedef unsigned long long u64;
#define LAS __attribute__((address_space(3)))
#define GAS __attribute__((address_space(1)))
#define RLX_AGENT __ATOMIC_RELAXED, __HIP_MEMORY_SCOPE_AGENT
#define SBAR() __builtin_amdgcn_sched_barrier(0)
#define MFMA(a, b, c) __builtin_amdgcn_mfma_f32_32x32x16_bf16(a, b, c, 0, 0, 0)
#define BARRIER() asm volatile("s_waitcnt lgkmcnt(0)\n\ts_barrier" ::: "memory")
#define VMWAIT(N) asm volatile("s_waitcnt vmcnt(" #N ")" ::: "memory")

constexpr unsigned WS_EPOCH = 0, WS_KVFLAG = 32768, WS_HFLAG = 65536, WS_OFLAG = 98304, WS_KV = 131072;
constexpr unsigned WS_G = 409600, WS_H = 614400, WS_O = WS_H + 64 * 86 * 1024, WS_END = WS_O + 64 * 32 * 1024;
constexpr unsigned KVT = 2112, VST = 544, KVB = 64 * KVT;
constexpr unsigned L_XN = 32 * KVT, L_KV0 = 64 * KVT, L_WK = L_KV0, L_WV = 49152, L_KVP = L_WK + 16384, L_KVS = L_KVP + 8192, L_W2 = L_KV0;
constexpr unsigned L_RED = 0, L_WO = 16384, L_CTRL = 163584, L_TOTAL = 163840;
constexpr unsigned C_PART = L_CTRL,
                   C_KNA = L_CTRL + 28,
                   C_MID = L_CTRL + 32,
                   C_WQ2 = L_CTRL + 24, C_PR = L_CTRL + 56, C_XN = L_CTRL + 64, C_KVS1 = L_CTRL + 68, C_KVS2 = L_CTRL + 72, C_KVD = L_CTRL + 76,
                   C_KVF = L_CTRL + 80, C_CF = L_CTRL + 84, C_QD = L_CTRL + 88, C_F1 = L_CTRL + 92, C_WO = L_CTRL + 96, C_KN0 = L_CTRL + 104,
                   C_KNR = L_CTRL + 108, C_TAG = L_CTRL + 112, C_HCNT = L_CTRL + 120, C_OCNT = L_CTRL + 124;
constexpr float QSCALE = 0.25f * 1.4426950408889634f;
constexpr float SAFE_BOUND = 40.0f;
constexpr unsigned CODE_BYTES = 41472;
constexpr unsigned SPIN_CAP = 1u << 16;


#define ATTN_LOOP_ASM \
  "ds_read_b128 v[112:115], %2 offset:0\n\t" \
  "ds_read_b128 v[124:127], %3 offset:2112\n\t" \
  "ds_read_b128 v[132:135], %3 offset:2656\n\t" \
  "ds_read_b128 v[116:119], %2 offset:2112\n\t" \
  "ds_read_b128 v[120:123], %3 offset:0\n\t" \
  "ds_read_b128 v[128:131], %3 offset:544\n\t" \
  "s_waitcnt lgkmcnt(5)\n\t" \
  "v_mfma_f32_32x32x16_bf16 v[136:151], v[112:115], %6, %8\n\t" \
  "v_mov_b32 v96, 0\n\t" \
  "v_mov_b32 v97, 0\n\t" \
  "v_mov_b32 v98, 0\n\t" \
  "v_mov_b32 v99, 0\n\t" \
  "v_mov_b32 v100, 0\n\t" \
  "v_mov_b32 v101, 0\n\t" \
  "v_mov_b32 v102, 0\n\t" \
  "v_mov_b32 v103, 0\n\t" \
  "v_mov_b32 v104, 0\n\t" \
  "v_mov_b32 v105, 0\n\t" \
  "v_mov_b32 v106, 0\n\t" \
  "v_mov_b32 v107, 0\n\t" \
  "v_mov_b32 v108, 0\n\t" \
  "v_mov_b32 v109, 0\n\t" \
  "v_mov_b32 v110, 0\n\t" \
  "v_mov_b32 v111, 0\n\t" \
  "s_waitcnt lgkmcnt(0)\n\t" \
  "v_mfma_f32_32x32x16_bf16 v[152:167], v[112:115], %7, %9\n\t" \
  "ds_read_b128 v[112:115], %2 offset:4224\n\t" \
  "v_exp_f32 v136, v136\n\t" \
  "v_exp_f32 v137, v137\n\t" \
  "v_exp_f32 v138, v138\n\t" \
  "v_smfmac_f32_32x32x32_bf16 %1, v[124:127], v[96:103], %10\n\t" \
  "ds_read_b128 v[124:127], %3 offset:2112\n\t" \
  "v_exp_f32 v139, v139\n\t" \
  "v_exp_f32 v140, v140\n\t" \
  "v_smfmac_f32_32x32x32_bf16 %1, v[132:135], v[104:111], %10\n\t" \
  "ds_read_b128 v[132:135], %3 offset:2656\n\t" \
  "v_exp_f32 v141, v141\n\t" \
  "v_exp_f32 v142, v142\n\t" \
  "v_exp_f32 v143, v143\n\t" \
  "v_exp_f32 v144, v144\n\t" \
  "v_exp_f32 v145, v145\n\t" \
  "v_exp_f32 v146, v146\n\t" \
  "v_exp_f32 v147, v147\n\t" \
  "v_exp_f32 v148, v148\n\t" \
  "v_exp_f32 v149, v149\n\t" \
  "v_exp_f32 v150, v150\n\t" \
  "v_exp_f32 v151, v151\n\t" \
  "v_mfma_f32_32x32x16_bf16 v[96:111], v[116:119], %6, %8\n\t" \
  "v_exp_f32 v152, v152\n\t" \
  "v_exp_f32 v153, v153\n\t" \
  "v_exp_f32 v154, v154\n\t" \
  "v_exp_f32 v155, v155\n\t" \
  "v_smfmac_f32_32x32x32_bf16 %0, v[120:123], v[136:143], %10\n\t" \
  "v_exp_f32 v156, v156\n\t" \
  "v_exp_f32 v157, v157\n\t" \
  "v_smfmac_f32_32x32x32_bf16 %0, v[128:131], v[144:151], %10\n\t" \
  "s_waitcnt lgkmcnt(0)\n\t" \
  "v_exp_f32 v158, v158\n\t" \
  "v_exp_f32 v159, v159\n\t" \
  "v_exp_f32 v160, v160\n\t" \
  "v_exp_f32 v161, v161\n\t" \
  "v_exp_f32 v162, v162\n\t" \
  "v_exp_f32 v163, v163\n\t" \
  "v_exp_f32 v164, v164\n\t" \
  "v_exp_f32 v165, v165\n\t" \
  "v_exp_f32 v166, v166\n\t" \
  "v_exp_f32 v167, v167\n\t" \
  "v_mfma_f32_32x32x16_bf16 v[136:151], v[116:119], %7, %9\n\t" \
  "ds_read_b128 v[116:119], %2 offset:6336\n\t" \
  "v_exp_f32 v96, v96\n\t" \
  "v_exp_f32 v97, v97\n\t" \
  "v_exp_f32 v98, v98\n\t" \
  "v_smfmac_f32_32x32x32_bf16 %1, v[120:123], v[152:159], %10\n\t" \
  "ds_read_b128 v[120:123], %3 offset:4224\n\t" \
  "v_exp_f32 v99, v99\n\t" \
  "v_exp_f32 v100, v100\n\t" \
  "v_smfmac_f32_32x32x32_bf16 %1, v[128:131], v[160:167], %10\n\t" \
  "ds_read_b128 v[128:131], %3 offset:4768\n\t" \
  "v_exp_f32 v101, v101\n\t" \
  "v_exp_f32 v102, v102\n\t" \
  "v_exp_f32 v103, v103\n\t" \
  "v_exp_f32 v104, v104\n\t" \
  "v_exp_f32 v105, v105\n\t" \
  "v_exp_f32 v106, v106\n\t" \
  "v_exp_f32 v107, v107\n\t" \
  "v_exp_f32 v108, v108\n\t" \
  "v_exp_f32 v109, v109\n\t" \
  "v_exp_f32 v110, v110\n\t" \
  "v_exp_f32 v111, v111\n\t" \
  "v_mfma_f32_32x32x16_bf16 v[152:167], v[112:115], %6, %8\n\t" \
  "v_exp_f32 v136, v136\n\t" \
  "v_exp_f32 v137, v137\n\t" \
  "v_exp_f32 v138, v138\n\t" \
  "v_exp_f32 v139, v139\n\t" \
  "v_smfmac_f32_32x32x32_bf16 %0, v[124:127], v[96:103], %10\n\t" \
  "v_exp_f32 v140, v140\n\t" \
  "v_exp_f32 v141, v141\n\t" \
  "v_smfmac_f32_32x32x32_bf16 %0, v[132:135], v[104:111], %10\n\t" \
  "s_waitcnt lgkmcnt(0)\n\t" \
  "v_exp_f32 v142, v142\n\t" \
  "v_exp_f32 v143, v143\n\t" \
  "v_exp_f32 v144, v144\n\t" \
  "v_exp_f32 v145, v145\n\t" \
  "v_exp_f32 v146, v146\n\t" \
  "v_exp_f32 v147, v147\n\t" \
  "v_exp_f32 v148, v148\n\t" \
  "v_exp_f32 v149, v149\n\t" \
  "v_exp_f32 v150, v150\n\t" \
  "v_exp_f32 v151, v151\n\t" \
  "v_mfma_f32_32x32x16_bf16 v[96:111], v[112:115], %7, %9\n\t" \
  "ds_read_b128 v[112:115], %2 offset:8448\n\t" \
  "v_exp_f32 v152, v152\n\t" \
  "v_exp_f32 v153, v153\n\t" \
  "v_exp_f32 v154, v154\n\t" \
  "v_smfmac_f32_32x32x32_bf16 %1, v[124:127], v[136:143], %10\n\t" \
  "ds_read_b128 v[124:127], %3 offset:6336\n\t" \
  "v_exp_f32 v155, v155\n\t" \
  "v_exp_f32 v156, v156\n\t" \
  "v_smfmac_f32_32x32x32_bf16 %1, v[132:135], v[144:151], %10\n\t" \
  "ds_read_b128 v[132:135], %3 offset:6880\n\t" \
  "v_exp_f32 v157, v157\n\t" \
  "v_exp_f32 v158, v158\n\t" \
  "v_exp_f32 v159, v159\n\t" \
  "v_exp_f32 v160, v160\n\t" \
  "v_exp_f32 v161, v161\n\t" \
  "v_exp_f32 v162, v162\n\t" \
  "v_exp_f32 v163, v163\n\t" \
  "v_exp_f32 v164, v164\n\t" \
  "v_exp_f32 v165, v165\n\t" \
  "v_exp_f32 v166, v166\n\t" \
  "v_exp_f32 v167, v167\n\t" \
  "v_mfma_f32_32x32x16_bf16 v[136:151], v[116:119], %6, %8\n\t" \
  "v_exp_f32 v96, v96\n\t" \
  "v_exp_f32 v97, v97\n\t" \
  "v_exp_f32 v98, v98\n\t" \
  "v_exp_f32 v99, v99\n\t" \
  "v_smfmac_f32_32x32x32_bf16 %0, v[120:123], v[152:159], %10\n\t" \
  "v_exp_f32 v100, v100\n\t" \
  "v_exp_f32 v101, v101\n\t" \
  "v_smfmac_f32_32x32x32_bf16 %0, v[128:131], v[160:167], %10\n\t" \
  "s_waitcnt lgkmcnt(0)\n\t" \
  "v_exp_f32 v102, v102\n\t" \
  "v_exp_f32 v103, v103\n\t" \
  "v_exp_f32 v104, v104\n\t" \
  "v_exp_f32 v105, v105\n\t" \
  "v_exp_f32 v106, v106\n\t" \
  "v_exp_f32 v107, v107\n\t" \
  "v_exp_f32 v108, v108\n\t" \
  "v_exp_f32 v109, v109\n\t" \
  "v_exp_f32 v110, v110\n\t" \
  "v_exp_f32 v111, v111\n\t" \
  "v_mfma_f32_32x32x16_bf16 v[152:167], v[116:119], %7, %9\n\t" \
  "ds_read_b128 v[116:119], %2 offset:10560\n\t" \
  "v_exp_f32 v136, v136\n\t" \
  "v_exp_f32 v137, v137\n\t" \
  "v_exp_f32 v138, v138\n\t" \
  "v_smfmac_f32_32x32x32_bf16 %1, v[120:123], v[96:103], %10\n\t" \
  "ds_read_b128 v[120:123], %3 offset:8448\n\t" \
  "v_exp_f32 v139, v139\n\t" \
  "v_exp_f32 v140, v140\n\t" \
  "v_smfmac_f32_32x32x32_bf16 %1, v[128:131], v[104:111], %10\n\t" \
  "ds_read_b128 v[128:131], %3 offset:8992\n\t" \
  "v_exp_f32 v141, v141\n\t" \
  "v_exp_f32 v142, v142\n\t" \
  "v_exp_f32 v143, v143\n\t" \
  "v_exp_f32 v144, v144\n\t" \
  "v_exp_f32 v145, v145\n\t" \
  "v_exp_f32 v146, v146\n\t" \
  "v_exp_f32 v147, v147\n\t" \
  "v_exp_f32 v148, v148\n\t" \
  "v_exp_f32 v149, v149\n\t" \
  "v_exp_f32 v150, v150\n\t" \
  "v_exp_f32 v151, v151\n\t" \
  "v_mfma_f32_32x32x16_bf16 v[96:111], v[112:115], %6, %8\n\t" \
  "ds_read_b32 %4, %11 offset:4\n\t" \
  "v_exp_f32 v152, v152\n\t" \
  "v_exp_f32 v153, v153\n\t" \
  "v_exp_f32 v154, v154\n\t" \
  "v_exp_f32 v155, v155\n\t" \
  "v_smfmac_f32_32x32x32_bf16 %0, v[124:127], v[136:143], %10\n\t" \
  "v_exp_f32 v156, v156\n\t" \
  "v_exp_f32 v157, v157\n\t" \
  "v_smfmac_f32_32x32x32_bf16 %0, v[132:135], v[144:151], %10\n\t" \
  "s_waitcnt lgkmcnt(0)\n\t" \
  "v_exp_f32 v158, v158\n\t" \
  "v_exp_f32 v159, v159\n\t" \
  "v_exp_f32 v160, v160\n\t" \
  "v_exp_f32 v161, v161\n\t" \
  "v_exp_f32 v162, v162\n\t" \
  "v_exp_f32 v163, v163\n\t" \
  "v_exp_f32 v164, v164\n\t" \
  "v_exp_f32 v165, v165\n\t" \
  "v_exp_f32 v166, v166\n\t" \
  "v_exp_f32 v167, v167\n\t" \
  "v_mfma_f32_32x32x16_bf16 v[136:151], v[112:115], %7, %9\n\t" \
  "v_readfirstlane_b32 %5, %4\n\t" \
  "s_nop 0\n\t" \
  "s_cmp_lt_u32 %5, 1\n\t" \
  "s_cbranch_scc1 11f\n\t" \
  "21:\n\t" \
  "ds_read_b32 %4, %11 offset:28\n\t" \
  "s_waitcnt lgkmcnt(0)\n\t" \
  "v_cmp_gt_u32 vcc, %4, %12\n\t" \
  "s_nop 4\n\t" \
  "s_cbranch_vccnz 8f\n\t" \
  "ds_read_b128 v[112:115], %2 offset:12672\n\t" \
  "v_exp_f32 v96, v96\n\t" \
  "v_exp_f32 v97, v97\n\t" \
  "v_exp_f32 v98, v98\n\t" \
  "v_smfmac_f32_32x32x32_bf16 %1, v[124:127], v[152:159], %10\n\t" \
  "ds_read_b128 v[124:127], %3 offset:10560\n\t" \
  "v_exp_f32 v99, v99\n\t" \
  "v_exp_f32 v100, v100\n\t" \
  "v_smfmac_f32_32x32x32_bf16 %1, v[132:135], v[160:167], %10\n\t" \
  "ds_read_b128 v[132:135], %3 offset:11104\n\t" \
  "v_exp_f32 v101, v101\n\t" \
  "v_exp_f32 v102, v102\n\t" \
  "v_exp_f32 v103, v103\n\t" \
  "v_exp_f32 v104, v104\n\t" \
  "v_exp_f32 v105, v105\n\t" \
  "v_exp_f32 v106, v106\n\t" \
  "v_exp_f32 v107, v107\n\t" \
  "v_exp_f32 v108, v108\n\t" \
  "v_exp_f32 v109, v109\n\t" \
  "v_exp_f32 v110, v110\n\t" \
  "v_exp_f32 v111, v111\n\t" \
  "v_mfma_f32_32x32x16_bf16 v[152:167], v[116:119], %6, %8\n\t" \
  "v_exp_f32 v136, v136\n\t" \
  "v_exp_f32 v137, v137\n\t" \
  "v_exp_f32 v138, v138\n\t" \
  "v_exp_f32 v139, v139\n\t" \
  "v_smfmac_f32_32x32x32_bf16 %0, v[120:123], v[96:103], %10\n\t" \
  "v_exp_f32 v140, v140\n\t" \
  "v_exp_f32 v141, v141\n\t" \
  "v_smfmac_f32_32x32x32_bf16 %0, v[128:131], v[104:111], %10\n\t" \
  "s_waitcnt lgkmcnt(0)\n\t" \
  "v_exp_f32 v142, v142\n\t" \
  "v_exp_f32 v143, v143\n\t" \
  "v_exp_f32 v144, v144\n\t" \
  "v_exp_f32 v145, v145\n\t" \
  "v_exp_f32 v146, v146\n\t" \
  "v_exp_f32 v147, v147\n\t" \
  "v_exp_f32 v148, v148\n\t" \
  "v_exp_f32 v149, v149\n\t" \
  "v_exp_f32 v150, v150\n\t" \
  "v_exp_f32 v151, v151\n\t" \
  "v_mfma_f32_32x32x16_bf16 v[96:111], v[116:119], %7, %9\n\t" \
  "ds_read_b128 v[116:119], %2 offset:14784\n\t" \
  "v_exp_f32 v152, v152\n\t" \
  "v_exp_f32 v153, v153\n\t" \
  "v_exp_f32 v154, v154\n\t" \
  "v_smfmac_f32_32x32x32_bf16 %1, v[120:123], v[136:143], %10\n\t" \
  "ds_read_b128 v[120:123], %3 offset:12672\n\t" \
  "v_exp_f32 v155, v155\n\t" \
  "v_exp_f32 v156, v156\n\t" \
  "v_smfmac_f32_32x32x32_bf16 %1, v[128:131], v[144:151], %10\n\t" \
  "ds_read_b128 v[128:131], %3 offset:13216\n\t" \
  "v_exp_f32 v157, v157\n\t" \
  "v_exp_f32 v158, v158\n\t" \
  "v_exp_f32 v159, v159\n\t" \
  "v_exp_f32 v160, v160\n\t" \
  "v_exp_f32 v161, v161\n\t" \
  "v_exp_f32 v162, v162\n\t" \
  "v_exp_f32 v163, v163\n\t" \
  "v_exp_f32 v164, v164\n\t" \
  "v_exp_f32 v165, v165\n\t" \
  "v_exp_f32 v166, v166\n\t" \
  "v_exp_f32 v167, v167\n\t" \
  "v_mfma_f32_32x32x16_bf16 v[136:151], v[112:115], %6, %8\n\t" \
  "v_exp_f32 v96, v96\n\t" \
  "v_exp_f32 v97, v97\n\t" \
  "v_exp_f32 v98, v98\n\t" \
  "v_exp_f32 v99, v99\n\t" \
  "v_smfmac_f32_32x32x32_bf16 %0, v[124:127], v[152:159], %10\n\t" \
  "v_exp_f32 v100, v100\n\t" \
  "v_exp_f32 v101, v101\n\t" \
  "v_smfmac_f32_32x32x32_bf16 %0, v[132:135], v[160:167], %10\n\t" \
  "s_waitcnt lgkmcnt(0)\n\t" \
  "v_exp_f32 v102, v102\n\t" \
  "v_exp_f32 v103, v103\n\t" \
  "v_exp_f32 v104, v104\n\t" \
  "v_exp_f32 v105, v105\n\t" \
  "v_exp_f32 v106, v106\n\t" \
  "v_exp_f32 v107, v107\n\t" \
  "v_exp_f32 v108, v108\n\t" \
  "v_exp_f32 v109, v109\n\t" \
  "v_exp_f32 v110, v110\n\t" \
  "v_exp_f32 v111, v111\n\t" \
  "v_mfma_f32_32x32x16_bf16 v[152:167], v[112:115], %7, %9\n\t" \
  "ds_read_b128 v[112:115], %2 offset:16896\n\t" \
  "v_exp_f32 v136, v136\n\t" \
  "v_exp_f32 v137, v137\n\t" \
  "v_exp_f32 v138, v138\n\t" \
  "v_smfmac_f32_32x32x32_bf16 %1, v[124:127], v[96:103], %10\n\t" \
  "ds_read_b128 v[124:127], %3 offset:14784\n\t" \
  "v_exp_f32 v139, v139\n\t" \
  "v_exp_f32 v140, v140\n\t" \
  "v_smfmac_f32_32x32x32_bf16 %1, v[132:135], v[104:111], %10\n\t" \
  "ds_read_b128 v[132:135], %3 offset:15328\n\t" \
  "v_exp_f32 v141, v141\n\t" \
  "v_exp_f32 v142, v142\n\t" \
  "v_exp_f32 v143, v143\n\t" \
  "v_exp_f32 v144, v144\n\t" \
  "v_exp_f32 v145, v145\n\t" \
  "v_exp_f32 v146, v146\n\t" \
  "v_exp_f32 v147, v147\n\t" \
  "v_exp_f32 v148, v148\n\t" \
  "v_exp_f32 v149, v149\n\t" \
  "v_exp_f32 v150, v150\n\t" \
  "v_exp_f32 v151, v151\n\t" \
  "v_mfma_f32_32x32x16_bf16 v[96:111], v[116:119], %6, %8\n\t" \
  "v_mov_b32 %4, 1\n\t" \
  "s_mov_b64 exec, 1\n\t" \
  "ds_add_u32 %11, %4 offset:36\n\t" \
  "s_mov_b64 exec, -1\n\t" \
  "v_exp_f32 v152, v152\n\t" \
  "v_exp_f32 v153, v153\n\t" \
  "v_exp_f32 v154, v154\n\t" \
  "v_exp_f32 v155, v155\n\t" \
  "v_smfmac_f32_32x32x32_bf16 %0, v[120:123], v[136:143], %10\n\t" \
  "v_exp_f32 v156, v156\n\t" \
  "v_exp_f32 v157, v157\n\t" \
  "v_smfmac_f32_32x32x32_bf16 %0, v[128:131], v[144:151], %10\n\t" \
  "s_waitcnt lgkmcnt(0)\n\t" \
  "v_exp_f32 v158, v158\n\t" \
  "v_exp_f32 v159, v159\n\t" \
  "v_exp_f32 v160, v160\n\t" \
  "v_exp_f32 v161, v161\n\t" \
  "v_exp_f32 v162, v162\n\t" \
  "v_exp_f32 v163, v163\n\t" \
  "v_exp_f32 v164, v164\n\t" \
  "v_exp_f32 v165, v165\n\t" \
  "v_exp_f32 v166, v166\n\t" \
  "v_exp_f32 v167, v167\n\t" \
  "v_mfma_f32_32x32x16_bf16 v[136:151], v[116:119], %7, %9\n\t" \
  "ds_read_b128 v[116:119], %2 offset:19008\n\t" \
  "v_exp_f32 v96, v96\n\t" \
  "v_exp_f32 v97, v97\n\t" \
  "v_exp_f32 v98, v98\n\t" \
  "v_smfmac_f32_32x32x32_bf16 %1, v[120:123], v[152:159], %10\n\t" \
  "ds_read_b128 v[120:123], %3 offset:16896\n\t" \
  "v_exp_f32 v99, v99\n\t" \
  "v_exp_f32 v100, v100\n\t" \
  "v_smfmac_f32_32x32x32_bf16 %1, v[128:131], v[160:167], %10\n\t" \
  "ds_read_b128 v[128:131], %3 offset:17440\n\t" \
  "v_exp_f32 v101, v101\n\t" \
  "v_exp_f32 v102, v102\n\t" \
  "v_exp_f32 v103, v103\n\t" \
  "v_exp_f32 v104, v104\n\t" \
  "v_exp_f32 v105, v105\n\t" \
  "v_exp_f32 v106, v106\n\t" \
  "v_exp_f32 v107, v107\n\t" \
  "v_exp_f32 v108, v108\n\t" \
  "v_exp_f32 v109, v109\n\t" \
  "v_exp_f32 v110, v110\n\t" \
  "v_exp_f32 v111, v111\n\t" \
  "v_mfma_f32_32x32x16_bf16 v[152:167], v[112:115], %6, %8\n\t" \
  "v_exp_f32 v136, v136\n\t" \
  "v_exp_f32 v137, v137\n\t" \
  "v_exp_f32 v138, v138\n\t" \
  "v_exp_f32 v139, v139\n\t" \
  "v_smfmac_f32_32x32x32_bf16 %0, v[124:127], v[96:103], %10\n\t" \
  "v_exp_f32 v140, v140\n\t" \
  "v_exp_f32 v141, v141\n\t" \
  "v_smfmac_f32_32x32x32_bf16 %0, v[132:135], v[104:111], %10\n\t" \
  "s_waitcnt lgkmcnt(0)\n\t" \
  "v_exp_f32 v142, v142\n\t" \
  "v_exp_f32 v143, v143\n\t" \
  "v_exp_f32 v144, v144\n\t" \
  "v_exp_f32 v145, v145\n\t" \
  "v_exp_f32 v146, v146\n\t" \
  "v_exp_f32 v147, v147\n\t" \
  "v_exp_f32 v148, v148\n\t" \
  "v_exp_f32 v149, v149\n\t" \
  "v_exp_f32 v150, v150\n\t" \
  "v_exp_f32 v151, v151\n\t" \
  "v_mfma_f32_32x32x16_bf16 v[96:111], v[112:115], %7, %9\n\t" \
  "ds_read_b128 v[112:115], %2 offset:21120\n\t" \
  "v_exp_f32 v152, v152\n\t" \
  "v_exp_f32 v153, v153\n\t" \
  "v_exp_f32 v154, v154\n\t" \
  "v_smfmac_f32_32x32x32_bf16 %1, v[124:127], v[136:143], %10\n\t" \
  "ds_read_b128 v[124:127], %3 offset:19008\n\t" \
  "v_exp_f32 v155, v155\n\t" \
  "v_exp_f32 v156, v156\n\t" \
  "v_smfmac_f32_32x32x32_bf16 %1, v[132:135], v[144:151], %10\n\t" \
  "ds_read_b128 v[132:135], %3 offset:19552\n\t" \
  "v_exp_f32 v157, v157\n\t" \
  "v_exp_f32 v158, v158\n\t" \
  "v_exp_f32 v159, v159\n\t" \
  "v_exp_f32 v160, v160\n\t" \
  "v_exp_f32 v161, v161\n\t" \
  "v_exp_f32 v162, v162\n\t" \
  "v_exp_f32 v163, v163\n\t" \
  "v_exp_f32 v164, v164\n\t" \
  "v_exp_f32 v165, v165\n\t" \
  "v_exp_f32 v166, v166\n\t" \
  "v_exp_f32 v167, v167\n\t" \
  "v_mfma_f32_32x32x16_bf16 v[136:151], v[116:119], %6, %8\n\t" \
  "v_exp_f32 v96, v96\n\t" \
  "v_exp_f32 v97, v97\n\t" \
  "v_exp_f32 v98, v98\n\t" \
  "v_exp_f32 v99, v99\n\t" \
  "v_smfmac_f32_32x32x32_bf16 %0, v[120:123], v[152:159], %10\n\t" \
  "v_exp_f32 v100, v100\n\t" \
  "v_exp_f32 v101, v101\n\t" \
  "v_smfmac_f32_32x32x32_bf16 %0, v[128:131], v[160:167], %10\n\t" \
  "s_waitcnt lgkmcnt(0)\n\t" \
  "v_exp_f32 v102, v102\n\t" \
  "v_exp_f32 v103, v103\n\t" \
  "v_exp_f32 v104, v104\n\t" \
  "v_exp_f32 v105, v105\n\t" \
  "v_exp_f32 v106, v106\n\t" \
  "v_exp_f32 v107, v107\n\t" \
  "v_exp_f32 v108, v108\n\t" \
  "v_exp_f32 v109, v109\n\t" \
  "v_exp_f32 v110, v110\n\t" \
  "v_exp_f32 v111, v111\n\t" \
  "v_mfma_f32_32x32x16_bf16 v[152:167], v[116:119], %7, %9\n\t" \
  "ds_read_b128 v[116:119], %2 offset:23232\n\t" \
  "v_exp_f32 v136, v136\n\t" \
  "v_exp_f32 v137, v137\n\t" \
  "v_exp_f32 v138, v138\n\t" \
  "v_smfmac_f32_32x32x32_bf16 %1, v[120:123], v[96:103], %10\n\t" \
  "ds_read_b128 v[120:123], %3 offset:21120\n\t" \
  "v_exp_f32 v139, v139\n\t" \
  "v_exp_f32 v140, v140\n\t" \
  "v_smfmac_f32_32x32x32_bf16 %1, v[128:131], v[104:111], %10\n\t" \
  "ds_read_b128 v[128:131], %3 offset:21664\n\t" \
  "v_exp_f32 v141, v141\n\t" \
  "v_exp_f32 v142, v142\n\t" \
  "v_exp_f32 v143, v143\n\t" \
  "v_exp_f32 v144, v144\n\t" \
  "v_exp_f32 v145, v145\n\t" \
  "v_exp_f32 v146, v146\n\t" \
  "v_exp_f32 v147, v147\n\t" \
  "v_exp_f32 v148, v148\n\t" \
  "v_exp_f32 v149, v149\n\t" \
  "v_exp_f32 v150, v150\n\t" \
  "v_exp_f32 v151, v151\n\t" \
  "v_mfma_f32_32x32x16_bf16 v[96:111], v[112:115], %6, %8\n\t" \
  "v_exp_f32 v152, v152\n\t" \
  "v_exp_f32 v153, v153\n\t" \
  "v_exp_f32 v154, v154\n\t" \
  "v_exp_f32 v155, v155\n\t" \
  "v_smfmac_f32_32x32x32_bf16 %0, v[124:127], v[136:143], %10\n\t" \
  "v_exp_f32 v156, v156\n\t" \
  "v_exp_f32 v157, v157\n\t" \
  "v_smfmac_f32_32x32x32_bf16 %0, v[132:135], v[144:151], %10\n\t" \
  "s_waitcnt lgkmcnt(0)\n\t" \
  "v_exp_f32 v158, v158\n\t" \
  "v_exp_f32 v159, v159\n\t" \
  "v_exp_f32 v160, v160\n\t" \
  "v_exp_f32 v161, v161\n\t" \
  "v_exp_f32 v162, v162\n\t" \
  "v_exp_f32 v163, v163\n\t" \
  "v_exp_f32 v164, v164\n\t" \
  "v_exp_f32 v165, v165\n\t" \
  "v_exp_f32 v166, v166\n\t" \
  "v_exp_f32 v167, v167\n\t" \
  "v_mfma_f32_32x32x16_bf16 v[136:151], v[112:115], %7, %9\n\t" \
  "ds_read_b128 v[112:115], %2 offset:25344\n\t" \
  "v_exp_f32 v96, v96\n\t" \
  "v_exp_f32 v97, v97\n\t" \
  "v_exp_f32 v98, v98\n\t" \
  "v_smfmac_f32_32x32x32_bf16 %1, v[124:127], v[152:159], %10\n\t" \
  "ds_read_b128 v[124:127], %3 offset:23232\n\t" \
  "v_exp_f32 v99, v99\n\t" \
  "v_exp_f32 v100, v100\n\t" \
  "v_smfmac_f32_32x32x32_bf16 %1, v[132:135], v[160:167], %10\n\t" \
  "ds_read_b128 v[132:135], %3 offset:23776\n\t" \
  "v_exp_f32 v101, v101\n\t" \
  "v_exp_f32 v102, v102\n\t" \
  "v_exp_f32 v103, v103\n\t" \
  "v_exp_f32 v104, v104\n\t" \
  "v_exp_f32 v105, v105\n\t" \
  "v_exp_f32 v106, v106\n\t" \
  "v_exp_f32 v107, v107\n\t" \
  "v_exp_f32 v108, v108\n\t" \
  "v_exp_f32 v109, v109\n\t" \
  "v_exp_f32 v110, v110\n\t" \
  "v_exp_f32 v111, v111\n\t" \
  "v_mfma_f32_32x32x16_bf16 v[152:167], v[116:119], %6, %8\n\t" \
  "ds_read_b32 %4, %11 offset:8\n\t" \
  "v_exp_f32 v136, v136\n\t" \
  "v_exp_f32 v137, v137\n\t" \
  "v_exp_f32 v138, v138\n\t" \
  "v_exp_f32 v139, v139\n\t" \
  "v_smfmac_f32_32x32x32_bf16 %0, v[120:123], v[96:103], %10\n\t" \
  "v_exp_f32 v140, v140\n\t" \
  "v_exp_f32 v141, v141\n\t" \
  "v_smfmac_f32_32x32x32_bf16 %0, v[128:131], v[104:111], %10\n\t" \
  "s_waitcnt lgkmcnt(0)\n\t" \
  "v_exp_f32 v142, v142\n\t" \
  "v_exp_f32 v143, v143\n\t" \
  "v_exp_f32 v144, v144\n\t" \
  "v_exp_f32 v145, v145\n\t" \
  "v_exp_f32 v146, v146\n\t" \
  "v_exp_f32 v147, v147\n\t" \
  "v_exp_f32 v148, v148\n\t" \
  "v_exp_f32 v149, v149\n\t" \
  "v_exp_f32 v150, v150\n\t" \
  "v_exp_f32 v151, v151\n\t" \
  "v_mfma_f32_32x32x16_bf16 v[96:111], v[116:119], %7, %9\n\t" \
  "v_readfirstlane_b32 %5, %4\n\t" \
  "s_nop 0\n\t" \
  "s_cmp_lt_u32 %5, 4\n\t" \
  "s_cbranch_scc1 12f\n\t" \
  "22:\n\t" \
  "v_add_u32 %2, 0xfffe3200, %2\n\t" \
  "ds_read_b128 v[116:119], %2 offset:0\n\t" \
  "v_exp_f32 v152, v152\n\t" \
  "v_exp_f32 v153, v153\n\t" \
  "v_exp_f32 v154, v154\n\t" \
  "v_smfmac_f32_32x32x32_bf16 %1, v[120:123], v[136:143], %10\n\t" \
  "ds_read_b128 v[120:123], %3 offset:25344\n\t" \
  "v_exp_f32 v155, v155\n\t" \
  "v_exp_f32 v156, v156\n\t" \
  "v_smfmac_f32_32x32x32_bf16 %1, v[128:131], v[144:151], %10\n\t" \
  "ds_read_b128 v[128:131], %3 offset:25888\n\t" \
  "v_exp_f32 v157, v157\n\t" \
  "v_exp_f32 v158, v158\n\t" \
  "v_exp_f32 v159, v159\n\t" \
  "v_exp_f32 v160, v160\n\t" \
  "v_exp_f32 v161, v161\n\t" \
  "v_exp_f32 v162, v162\n\t" \
  "v_exp_f32 v163, v163\n\t" \
  "v_exp_f32 v164, v164\n\t" \
  "v_exp_f32 v165, v165\n\t" \
  "v_exp_f32 v166, v166\n\t" \
  "v_exp_f32 v167, v167\n\t" \
  "v_mfma_f32_32x32x16_bf16 v[136:151], v[112:115], %6, %8\n\t" \
  "v_exp_f32 v96, v96\n\t" \
  "v_exp_f32 v97, v97\n\t" \
  "v_exp_f32 v98, v98\n\t" \
  "v_exp_f32 v99, v99\n\t" \
  "v_smfmac_f32_32x32x32_bf16 %0, v[124:127], v[152:159], %10\n\t" \
  "v_exp_f32 v100, v100\n\t" \
  "v_exp_f32 v101, v101\n\t" \
  "v_smfmac_f32_32x32x32_bf16 %0, v[132:135], v[160:167], %10\n\t" \
  "s_waitcnt lgkmcnt(0)\n\t" \
  "v_exp_f32 v102, v102\n\t" \
  "v_exp_f32 v103, v103\n\t" \
  "v_exp_f32 v104, v104\n\t" \
  "v_exp_f32 v105, v105\n\t" \
  "v_exp_f32 v106, v106\n\t" \
  "v_exp_f32 v107, v107\n\t" \
  "v_exp_f32 v108, v108\n\t" \
  "v_exp_f32 v109, v109\n\t" \
  "v_exp_f32 v110, v110\n\t" \
  "v_exp_f32 v111, v111\n\t" \
  "v_mfma_f32_32x32x16_bf16 v[152:167], v[112:115], %7, %9\n\t" \
  "ds_read_b128 v[112:115], %2 offset:2112\n\t" \
  "v_exp_f32 v136, v136\n\t" \
  "v_exp_f32 v137, v137\n\t" \
  "v_exp_f32 v138, v138\n\t" \
  "v_smfmac_f32_32x32x32_bf16 %1, v[124:127], v[96:103], %10\n\t" \
  "v_add_u32 %3, 0xfffe3200, %3\n\t" \
  "ds_read_b128 v[124:127], %3 offset:0\n\t" \
  "v_exp_f32 v139, v139\n\t" \
  "v_exp_f32 v140, v140\n\t" \
  "v_smfmac_f32_32x32x32_bf16 %1, v[132:135], v[104:111], %10\n\t" \
  "ds_read_b128 v[132:135], %3 offset:544\n\t" \
  "v_exp_f32 v141, v141\n\t" \
  "v_exp_f32 v142, v142\n\t" \
  "v_exp_f32 v143, v143\n\t" \
  "v_exp_f32 v144, v144\n\t" \
  "v_exp_f32 v145, v145\n\t" \
  "v_exp_f32 v146, v146\n\t" \
  "v_exp_f32 v147, v147\n\t" \
  "v_exp_f32 v148, v148\n\t" \
  "v_exp_f32 v149, v149\n\t" \
  "v_exp_f32 v150, v150\n\t" \
  "v_exp_f32 v151, v151\n\t" \
  "v_mfma_f32_32x32x16_bf16 v[96:111], v[116:119], %6, %8\n\t" \
  "v_exp_f32 v152, v152\n\t" \
  "v_exp_f32 v153, v153\n\t" \
  "v_exp_f32 v154, v154\n\t" \
  "v_exp_f32 v155, v155\n\t" \
  "v_smfmac_f32_32x32x32_bf16 %0, v[120:123], v[136:143], %10\n\t" \
  "v_exp_f32 v156, v156\n\t" \
  "v_exp_f32 v157, v157\n\t" \
  "v_smfmac_f32_32x32x32_bf16 %0, v[128:131], v[144:151], %10\n\t" \
  "s_waitcnt lgkmcnt(0)\n\t" \
  "v_exp_f32 v158, v158\n\t" \
  "v_exp_f32 v159, v159\n\t" \
  "v_exp_f32 v160, v160\n\t" \
  "v_exp_f32 v161, v161\n\t" \
  "v_exp_f32 v162, v162\n\t" \
  "v_exp_f32 v163, v163\n\t" \
  "v_exp_f32 v164, v164\n\t" \
  "v_exp_f32 v165, v165\n\t" \
  "v_exp_f32 v166, v166\n\t" \
  "v_exp_f32 v167, v167\n\t" \
  "v_mfma_f32_32x32x16_bf16 v[136:151], v[116:119], %7, %9\n\t" \
  "ds_read_b128 v[116:119], %2 offset:4224\n\t" \
  "v_exp_f32 v96, v96\n\t" \
  "v_exp_f32 v97, v97\n\t" \
  "v_exp_f32 v98, v98\n\t" \
  "v_smfmac_f32_32x32x32_bf16 %1, v[120:123], v[152:159], %10\n\t" \
  "ds_read_b128 v[120:123], %3 offset:2112\n\t" \
  "v_exp_f32 v99, v99\n\t" \
  "v_exp_f32 v100, v100\n\t" \
  "v_smfmac_f32_32x32x32_bf16 %1, v[128:131], v[160:167], %10\n\t" \
  "ds_read_b128 v[128:131], %3 offset:2656\n\t" \
  "v_exp_f32 v101, v101\n\t" \
  "v_exp_f32 v102, v102\n\t" \
  "v_exp_f32 v103, v103\n\t" \
  "v_exp_f32 v104, v104\n\t" \
  "v_exp_f32 v105, v105\n\t" \
  "v_exp_f32 v106, v106\n\t" \
  "v_exp_f32 v107, v107\n\t" \
  "v_exp_f32 v108, v108\n\t" \
  "v_exp_f32 v109, v109\n\t" \
  "v_exp_f32 v110, v110\n\t" \
  "v_exp_f32 v111, v111\n\t" \
  "v_mfma_f32_32x32x16_bf16 v[152:167], v[112:115], %6, %8\n\t" \
  "v_mov_b32 %4, 1\n\t" \
  "s_mov_b64 exec, 1\n\t" \
  "ds_add_u32 %11, %4 offset:40\n\t" \
  "s_mov_b64 exec, -1\n\t" \
  "v_exp_f32 v136, v136\n\t" \
  "v_exp_f32 v137, v137\n\t" \
  "v_exp_f32 v138, v138\n\t" \
  "v_exp_f32 v139, v139\n\t" \
  "v_smfmac_f32_32x32x32_bf16 %0, v[124:127], v[96:103], %10\n\t" \
  "v_exp_f32 v140, v140\n\t" \
  "v_exp_f32 v141, v141\n\t" \
  "v_smfmac_f32_32x32x32_bf16 %0, v[132:135], v[104:111], %10\n\t" \
  "s_waitcnt lgkmcnt(0)\n\t" \
  "v_exp_f32 v142, v142\n\t" \
  "v_exp_f32 v143, v143\n\t" \
  "v_exp_f32 v144, v144\n\t" \
  "v_exp_f32 v145, v145\n\t" \
  "v_exp_f32 v146, v146\n\t" \
  "v_exp_f32 v147, v147\n\t" \
  "v_exp_f32 v148, v148\n\t" \
  "v_exp_f32 v149, v149\n\t" \
  "v_exp_f32 v150, v150\n\t" \
  "v_exp_f32 v151, v151\n\t" \
  "v_mfma_f32_32x32x16_bf16 v[96:111], v[112:115], %7, %9\n\t" \
  "ds_read_b128 v[112:115], %2 offset:6336\n\t" \
  "v_exp_f32 v152, v152\n\t" \
  "v_exp_f32 v153, v153\n\t" \
  "v_exp_f32 v154, v154\n\t" \
  "v_smfmac_f32_32x32x32_bf16 %1, v[124:127], v[136:143], %10\n\t" \
  "ds_read_b128 v[124:127], %3 offset:4224\n\t" \
  "v_exp_f32 v155, v155\n\t" \
  "v_exp_f32 v156, v156\n\t" \
  "v_smfmac_f32_32x32x32_bf16 %1, v[132:135], v[144:151], %10\n\t" \
  "ds_read_b128 v[132:135], %3 offset:4768\n\t" \
  "v_exp_f32 v157, v157\n\t" \
  "v_exp_f32 v158, v158\n\t" \
  "v_exp_f32 v159, v159\n\t" \
  "v_exp_f32 v160, v160\n\t" \
  "v_exp_f32 v161, v161\n\t" \
  "v_exp_f32 v162, v162\n\t" \
  "v_exp_f32 v163, v163\n\t" \
  "v_exp_f32 v164, v164\n\t" \
  "v_exp_f32 v165, v165\n\t" \
  "v_exp_f32 v166, v166\n\t" \
  "v_exp_f32 v167, v167\n\t" \
  "v_mfma_f32_32x32x16_bf16 v[136:151], v[116:119], %6, %8\n\t" \
  "v_exp_f32 v96, v96\n\t" \
  "v_exp_f32 v97, v97\n\t" \
  "v_exp_f32 v98, v98\n\t" \
  "v_exp_f32 v99, v99\n\t" \
  "v_smfmac_f32_32x32x32_bf16 %0, v[120:123], v[152:159], %10\n\t" \
  "v_exp_f32 v100, v100\n\t" \
  "v_exp_f32 v101, v101\n\t" \
  "v_smfmac_f32_32x32x32_bf16 %0, v[128:131], v[160:167], %10\n\t" \
  "s_waitcnt lgkmcnt(0)\n\t" \
  "v_exp_f32 v102, v102\n\t" \
  "v_exp_f32 v103, v103\n\t" \
  "v_exp_f32 v104, v104\n\t" \
  "v_exp_f32 v105, v105\n\t" \
  "v_exp_f32 v106, v106\n\t" \
  "v_exp_f32 v107, v107\n\t" \
  "v_exp_f32 v108, v108\n\t" \
  "v_exp_f32 v109, v109\n\t" \
  "v_exp_f32 v110, v110\n\t" \
  "v_exp_f32 v111, v111\n\t" \
  "v_mfma_f32_32x32x16_bf16 v[152:167], v[116:119], %7, %9\n\t" \
  "ds_read_b128 v[116:119], %2 offset:8448\n\t" \
  "v_exp_f32 v136, v136\n\t" \
  "v_exp_f32 v137, v137\n\t" \
  "v_exp_f32 v138, v138\n\t" \
  "v_smfmac_f32_32x32x32_bf16 %1, v[120:123], v[96:103], %10\n\t" \
  "ds_read_b128 v[120:123], %3 offset:6336\n\t" \
  "v_exp_f32 v139, v139\n\t" \
  "v_exp_f32 v140, v140\n\t" \
  "v_smfmac_f32_32x32x32_bf16 %1, v[128:131], v[104:111], %10\n\t" \
  "ds_read_b128 v[128:131], %3 offset:6880\n\t" \
  "v_exp_f32 v141, v141\n\t" \
  "v_exp_f32 v142, v142\n\t" \
  "v_exp_f32 v143, v143\n\t" \
  "v_exp_f32 v144, v144\n\t" \
  "v_exp_f32 v145, v145\n\t" \
  "v_exp_f32 v146, v146\n\t" \
  "v_exp_f32 v147, v147\n\t" \
  "v_exp_f32 v148, v148\n\t" \
  "v_exp_f32 v149, v149\n\t" \
  "v_exp_f32 v150, v150\n\t" \
  "v_exp_f32 v151, v151\n\t" \
  "v_mfma_f32_32x32x16_bf16 v[96:111], v[112:115], %6, %8\n\t" \
  "v_exp_f32 v152, v152\n\t" \
  "v_exp_f32 v153, v153\n\t" \
  "v_exp_f32 v154, v154\n\t" \
  "v_exp_f32 v155, v155\n\t" \
  "v_smfmac_f32_32x32x32_bf16 %0, v[124:127], v[136:143], %10\n\t" \
  "v_exp_f32 v156, v156\n\t" \
  "v_exp_f32 v157, v157\n\t" \
  "v_smfmac_f32_32x32x32_bf16 %0, v[132:135], v[144:151], %10\n\t" \
  "s_waitcnt lgkmcnt(0)\n\t" \
  "v_exp_f32 v158, v158\n\t" \
  "v_exp_f32 v159, v159\n\t" \
  "v_exp_f32 v160, v160\n\t" \
  "v_exp_f32 v161, v161\n\t" \
  "v_exp_f32 v162, v162\n\t" \
  "v_exp_f32 v163, v163\n\t" \
  "v_exp_f32 v164, v164\n\t" \
  "v_exp_f32 v165, v165\n\t" \
  "v_exp_f32 v166, v166\n\t" \
  "v_exp_f32 v167, v167\n\t" \
  "v_mfma_f32_32x32x16_bf16 v[136:151], v[112:115], %7, %9\n\t" \
  "ds_read_b128 v[112:115], %2 offset:10560\n\t" \
  "v_exp_f32 v96, v96\n\t" \
  "v_exp_f32 v97, v97\n\t" \
  "v_exp_f32 v98, v98\n\t" \
  "v_smfmac_f32_32x32x32_bf16 %1, v[124:127], v[152:159], %10\n\t" \
  "ds_read_b128 v[124:127], %3 offset:8448\n\t" \
  "v_exp_f32 v99, v99\n\t" \
  "v_exp_f32 v100, v100\n\t" \
  "v_smfmac_f32_32x32x32_bf16 %1, v[132:135], v[160:167], %10\n\t" \
  "ds_read_b128 v[132:135], %3 offset:8992\n\t" \
  "v_exp_f32 v101, v101\n\t" \
  "v_exp_f32 v102, v102\n\t" \
  "v_exp_f32 v103, v103\n\t" \
  "v_exp_f32 v104, v104\n\t" \
  "v_exp_f32 v105, v105\n\t" \
  "v_exp_f32 v106, v106\n\t" \
  "v_exp_f32 v107, v107\n\t" \
  "v_exp_f32 v108, v108\n\t" \
  "v_exp_f32 v109, v109\n\t" \
  "v_exp_f32 v110, v110\n\t" \
  "v_exp_f32 v111, v111\n\t" \
  "v_mfma_f32_32x32x16_bf16 v[152:167], v[116:119], %6, %8\n\t" \
  "v_exp_f32 v136, v136\n\t" \
  "v_exp_f32 v137, v137\n\t" \
  "v_exp_f32 v138, v138\n\t" \
  "v_exp_f32 v139, v139\n\t" \
  "v_smfmac_f32_32x32x32_bf16 %0, v[120:123], v[96:103], %10\n\t" \
  "v_exp_f32 v140, v140\n\t" \
  "v_exp_f32 v141, v141\n\t" \
  "v_smfmac_f32_32x32x32_bf16 %0, v[128:131], v[104:111], %10\n\t" \
  "s_waitcnt lgkmcnt(0)\n\t" \
  "v_exp_f32 v142, v142\n\t" \
  "v_exp_f32 v143, v143\n\t" \
  "v_exp_f32 v144, v144\n\t" \
  "v_exp_f32 v145, v145\n\t" \
  "v_exp_f32 v146, v146\n\t" \
  "v_exp_f32 v147, v147\n\t" \
  "v_exp_f32 v148, v148\n\t" \
  "v_exp_f32 v149, v149\n\t" \
  "v_exp_f32 v150, v150\n\t" \
  "v_exp_f32 v151, v151\n\t" \
  "v_mfma_f32_32x32x16_bf16 v[96:111], v[116:119], %7, %9\n\t" \
  "ds_read_b128 v[116:119], %2 offset:12672\n\t" \
  "v_exp_f32 v152, v152\n\t" \
  "v_exp_f32 v153, v153\n\t" \
  "v_exp_f32 v154, v154\n\t" \
  "v_smfmac_f32_32x32x32_bf16 %1, v[120:123], v[136:143], %10\n\t" \
  "ds_read_b128 v[120:123], %3 offset:10560\n\t" \
  "v_exp_f32 v155, v155\n\t" \
  "v_exp_f32 v156, v156\n\t" \
  "v_smfmac_f32_32x32x32_bf16 %1, v[128:131], v[144:151], %10\n\t" \
  "ds_read_b128 v[128:131], %3 offset:11104\n\t" \
  "v_exp_f32 v157, v157\n\t" \
  "v_exp_f32 v158, v158\n\t" \
  "v_exp_f32 v159, v159\n\t" \
  "v_exp_f32 v160, v160\n\t" \
  "v_exp_f32 v161, v161\n\t" \
  "v_exp_f32 v162, v162\n\t" \
  "v_exp_f32 v163, v163\n\t" \
  "v_exp_f32 v164, v164\n\t" \
  "v_exp_f32 v165, v165\n\t" \
  "v_exp_f32 v166, v166\n\t" \
  "v_exp_f32 v167, v167\n\t" \
  "v_mfma_f32_32x32x16_bf16 v[136:151], v[112:115], %6, %8\n\t" \
  "v_exp_f32 v96, v96\n\t" \
  "v_exp_f32 v97, v97\n\t" \
  "v_exp_f32 v98, v98\n\t" \
  "v_exp_f32 v99, v99\n\t" \
  "v_smfmac_f32_32x32x32_bf16 %0, v[124:127], v[152:159], %10\n\t" \
  "v_exp_f32 v100, v100\n\t" \
  "v_exp_f32 v101, v101\n\t" \
  "v_smfmac_f32_32x32x32_bf16 %0, v[132:135], v[160:167], %10\n\t" \
  "s_waitcnt lgkmcnt(0)\n\t" \
  "v_exp_f32 v102, v102\n\t" \
  "v_exp_f32 v103, v103\n\t" \
  "v_exp_f32 v104, v104\n\t" \
  "v_exp_f32 v105, v105\n\t" \
  "v_exp_f32 v106, v106\n\t" \
  "v_exp_f32 v107, v107\n\t" \
  "v_exp_f32 v108, v108\n\t" \
  "v_exp_f32 v109, v109\n\t" \
  "v_exp_f32 v110, v110\n\t" \
  "v_exp_f32 v111, v111\n\t" \
  "v_mfma_f32_32x32x16_bf16 v[152:167], v[112:115], %7, %9\n\t" \
  "ds_read_b128 v[112:115], %2 offset:14784\n\t" \
  "v_exp_f32 v136, v136\n\t" \
  "v_exp_f32 v137, v137\n\t" \
  "v_exp_f32 v138, v138\n\t" \
  "v_smfmac_f32_32x32x32_bf16 %1, v[124:127], v[96:103], %10\n\t" \
  "ds_read_b128 v[124:127], %3 offset:12672\n\t" \
  "v_exp_f32 v139, v139\n\t" \
  "v_exp_f32 v140, v140\n\t" \
  "v_smfmac_f32_32x32x32_bf16 %1, v[132:135], v[104:111], %10\n\t" \
  "ds_read_b128 v[132:135], %3 offset:13216\n\t" \
  "v_exp_f32 v141, v141\n\t" \
  "v_exp_f32 v142, v142\n\t" \
  "v_exp_f32 v143, v143\n\t" \
  "v_exp_f32 v144, v144\n\t" \
  "v_exp_f32 v145, v145\n\t" \
  "v_exp_f32 v146, v146\n\t" \
  "v_exp_f32 v147, v147\n\t" \
  "v_exp_f32 v148, v148\n\t" \
  "v_exp_f32 v149, v149\n\t" \
  "v_exp_f32 v150, v150\n\t" \
  "v_exp_f32 v151, v151\n\t" \
  "v_mfma_f32_32x32x16_bf16 v[96:111], v[116:119], %6, %8\n\t" \
  "v_exp_f32 v152, v152\n\t" \
  "v_exp_f32 v153, v153\n\t" \
  "v_exp_f32 v154, v154\n\t" \
  "v_exp_f32 v155, v155\n\t" \
  "v_smfmac_f32_32x32x32_bf16 %0, v[120:123], v[136:143], %10\n\t" \
  "v_exp_f32 v156, v156\n\t" \
  "v_exp_f32 v157, v157\n\t" \
  "v_smfmac_f32_32x32x32_bf16 %0, v[128:131], v[144:151], %10\n\t" \
  "s_waitcnt lgkmcnt(0)\n\t" \
  "v_exp_f32 v158, v158\n\t" \
  "v_exp_f32 v159, v159\n\t" \
  "v_exp_f32 v160, v160\n\t" \
  "v_exp_f32 v161, v161\n\t" \
  "v_exp_f32 v162, v162\n\t" \
  "v_exp_f32 v163, v163\n\t" \
  "v_exp_f32 v164, v164\n\t" \
  "v_exp_f32 v165, v165\n\t" \
  "v_exp_f32 v166, v166\n\t" \
  "v_exp_f32 v167, v167\n\t" \
  "v_mfma_f32_32x32x16_bf16 v[136:151], v[116:119], %7, %9\n\t" \
  "ds_read_b128 v[116:119], %2 offset:16896\n\t" \
  "v_exp_f32 v96, v96\n\t" \
  "v_exp_f32 v97, v97\n\t" \
  "v_exp_f32 v98, v98\n\t" \
  "v_smfmac_f32_32x32x32_bf16 %1, v[120:123], v[152:159], %10\n\t" \
  "ds_read_b128 v[120:123], %3 offset:14784\n\t" \
  "v_exp_f32 v99, v99\n\t" \
  "v_exp_f32 v100, v100\n\t" \
  "v_smfmac_f32_32x32x32_bf16 %1, v[128:131], v[160:167], %10\n\t" \
  "ds_read_b128 v[128:131], %3 offset:15328\n\t" \
  "v_exp_f32 v101, v101\n\t" \
  "v_exp_f32 v102, v102\n\t" \
  "v_exp_f32 v103, v103\n\t" \
  "v_exp_f32 v104, v104\n\t" \
  "v_exp_f32 v105, v105\n\t" \
  "v_exp_f32 v106, v106\n\t" \
  "v_exp_f32 v107, v107\n\t" \
  "v_exp_f32 v108, v108\n\t" \
  "v_exp_f32 v109, v109\n\t" \
  "v_exp_f32 v110, v110\n\t" \
  "v_exp_f32 v111, v111\n\t" \
  "v_mfma_f32_32x32x16_bf16 v[152:167], v[112:115], %6, %8\n\t" \
  "v_exp_f32 v136, v136\n\t" \
  "v_exp_f32 v137, v137\n\t" \
  "v_exp_f32 v138, v138\n\t" \
  "v_exp_f32 v139, v139\n\t" \
  "v_smfmac_f32_32x32x32_bf16 %0, v[124:127], v[96:103], %10\n\t" \
  "v_exp_f32 v140, v140\n\t" \
  "v_exp_f32 v141, v141\n\t" \
  "v_smfmac_f32_32x32x32_bf16 %0, v[132:135], v[104:111], %10\n\t" \
  "s_waitcnt lgkmcnt(0)\n\t" \
  "v_exp_f32 v142, v142\n\t" \
  "v_exp_f32 v143, v143\n\t" \
  "v_exp_f32 v144, v144\n\t" \
  "v_exp_f32 v145, v145\n\t" \
  "v_exp_f32 v146, v146\n\t" \
  "v_exp_f32 v147, v147\n\t" \
  "v_exp_f32 v148, v148\n\t" \
  "v_exp_f32 v149, v149\n\t" \
  "v_exp_f32 v150, v150\n\t" \
  "v_exp_f32 v151, v151\n\t" \
  "v_mfma_f32_32x32x16_bf16 v[96:111], v[112:115], %7, %9\n\t" \
  "ds_read_b128 v[112:115], %2 offset:19008\n\t" \
  "v_exp_f32 v152, v152\n\t" \
  "v_exp_f32 v153, v153\n\t" \
  "v_exp_f32 v154, v154\n\t" \
  "v_smfmac_f32_32x32x32_bf16 %1, v[124:127], v[136:143], %10\n\t" \
  "ds_read_b128 v[124:127], %3 offset:16896\n\t" \
  "v_exp_f32 v155, v155\n\t" \
  "v_exp_f32 v156, v156\n\t" \
  "v_smfmac_f32_32x32x32_bf16 %1, v[132:135], v[144:151], %10\n\t" \
  "ds_read_b128 v[132:135], %3 offset:17440\n\t" \
  "v_exp_f32 v157, v157\n\t" \
  "v_exp_f32 v158, v158\n\t" \
  "v_exp_f32 v159, v159\n\t" \
  "v_exp_f32 v160, v160\n\t" \
  "v_exp_f32 v161, v161\n\t" \
  "v_exp_f32 v162, v162\n\t" \
  "v_exp_f32 v163, v163\n\t" \
  "v_exp_f32 v164, v164\n\t" \
  "v_exp_f32 v165, v165\n\t" \
  "v_exp_f32 v166, v166\n\t" \
  "v_exp_f32 v167, v167\n\t" \
  "v_mfma_f32_32x32x16_bf16 v[136:151], v[116:119], %6, %8\n\t" \
  "v_exp_f32 v96, v96\n\t" \
  "v_exp_f32 v97, v97\n\t" \
  "v_exp_f32 v98, v98\n\t" \
  "v_exp_f32 v99, v99\n\t" \
  "v_smfmac_f32_32x32x32_bf16 %0, v[120:123], v[152:159], %10\n\t" \
  "v_exp_f32 v100, v100\n\t" \
  "v_exp_f32 v101, v101\n\t" \
  "v_smfmac_f32_32x32x32_bf16 %0, v[128:131], v[160:167], %10\n\t" \
  "s_waitcnt lgkmcnt(0)\n\t" \
  "v_exp_f32 v102, v102\n\t" \
  "v_exp_f32 v103, v103\n\t" \
  "v_exp_f32 v104, v104\n\t" \
  "v_exp_f32 v105, v105\n\t" \
  "v_exp_f32 v106, v106\n\t" \
  "v_exp_f32 v107, v107\n\t" \
  "v_exp_f32 v108, v108\n\t" \
  "v_exp_f32 v109, v109\n\t" \
  "v_exp_f32 v110, v110\n\t" \
  "v_exp_f32 v111, v111\n\t" \
  "v_mfma_f32_32x32x16_bf16 v[152:167], v[116:119], %7, %9\n\t" \
  "ds_read_b128 v[116:119], %2 offset:21120\n\t" \
  "v_exp_f32 v136, v136\n\t" \
  "v_exp_f32 v137, v137\n\t" \
  "v_exp_f32 v138, v138\n\t" \
  "v_smfmac_f32_32x32x32_bf16 %1, v[120:123], v[96:103], %10\n\t" \
  "ds_read_b128 v[120:123], %3 offset:19008\n\t" \
  "v_exp_f32 v139, v139\n\t" \
  "v_exp_f32 v140, v140\n\t" \
  "v_smfmac_f32_32x32x32_bf16 %1, v[128:131], v[104:111], %10\n\t" \
  "ds_read_b128 v[128:131], %3 offset:19552\n\t" \
  "v_exp_f32 v141, v141\n\t" \
  "v_exp_f32 v142, v142\n\t" \
  "v_exp_f32 v143, v143\n\t" \
  "v_exp_f32 v144, v144\n\t" \
  "v_exp_f32 v145, v145\n\t" \
  "v_exp_f32 v146, v146\n\t" \
  "v_exp_f32 v147, v147\n\t" \
  "v_exp_f32 v148, v148\n\t" \
  "v_exp_f32 v149, v149\n\t" \
  "v_exp_f32 v150, v150\n\t" \
  "v_exp_f32 v151, v151\n\t" \
  "v_mfma_f32_32x32x16_bf16 v[96:111], v[112:115], %6, %8\n\t" \
  "v_exp_f32 v152, v152\n\t" \
  "v_exp_f32 v153, v153\n\t" \
  "v_exp_f32 v154, v154\n\t" \
  "v_exp_f32 v155, v155\n\t" \
  "v_smfmac_f32_32x32x32_bf16 %0, v[124:127], v[136:143], %10\n\t" \
  "v_exp_f32 v156, v156\n\t" \
  "v_exp_f32 v157, v157\n\t" \
  "v_smfmac_f32_32x32x32_bf16 %0, v[132:135], v[144:151], %10\n\t" \
  "s_waitcnt lgkmcnt(0)\n\t" \
  "v_exp_f32 v158, v158\n\t" \
  "v_exp_f32 v159, v159\n\t" \
  "v_exp_f32 v160, v160\n\t" \
  "v_exp_f32 v161, v161\n\t" \
  "v_exp_f32 v162, v162\n\t" \
  "v_exp_f32 v163, v163\n\t" \
  "v_exp_f32 v164, v164\n\t" \
  "v_exp_f32 v165, v165\n\t" \
  "v_exp_f32 v166, v166\n\t" \
  "v_exp_f32 v167, v167\n\t" \
  "v_mfma_f32_32x32x16_bf16 v[136:151], v[112:115], %7, %9\n\t" \
  "ds_read_b128 v[112:115], %2 offset:23232\n\t" \
  "v_exp_f32 v96, v96\n\t" \
  "v_exp_f32 v97, v97\n\t" \
  "v_exp_f32 v98, v98\n\t" \
  "v_smfmac_f32_32x32x32_bf16 %1, v[124:127], v[152:159], %10\n\t" \
  "ds_read_b128 v[124:127], %3 offset:21120\n\t" \
  "v_exp_f32 v99, v99\n\t" \
  "v_exp_f32 v100, v100\n\t" \
  "v_smfmac_f32_32x32x32_bf16 %1, v[132:135], v[160:167], %10\n\t" \
  "ds_read_b128 v[132:135], %3 offset:21664\n\t" \
  "v_exp_f32 v101, v101\n\t" \
  "v_exp_f32 v102, v102\n\t" \
  "v_exp_f32 v103, v103\n\t" \
  "v_exp_f32 v104, v104\n\t" \
  "v_exp_f32 v105, v105\n\t" \
  "v_exp_f32 v106, v106\n\t" \
  "v_exp_f32 v107, v107\n\t" \
  "v_exp_f32 v108, v108\n\t" \
  "v_exp_f32 v109, v109\n\t" \
  "v_exp_f32 v110, v110\n\t" \
  "v_exp_f32 v111, v111\n\t" \
  "v_mfma_f32_32x32x16_bf16 v[152:167], v[116:119], %6, %8\n\t" \
  "v_exp_f32 v136, v136\n\t" \
  "v_exp_f32 v137, v137\n\t" \
  "v_exp_f32 v138, v138\n\t" \
  "v_exp_f32 v139, v139\n\t" \
  "v_smfmac_f32_32x32x32_bf16 %0, v[120:123], v[96:103], %10\n\t" \
  "v_exp_f32 v140, v140\n\t" \
  "v_exp_f32 v141, v141\n\t" \
  "v_smfmac_f32_32x32x32_bf16 %0, v[128:131], v[104:111], %10\n\t" \
  "s_waitcnt lgkmcnt(0)\n\t" \
  "v_exp_f32 v142, v142\n\t" \
  "v_exp_f32 v143, v143\n\t" \
  "v_exp_f32 v144, v144\n\t" \
  "v_exp_f32 v145, v145\n\t" \
  "v_exp_f32 v146, v146\n\t" \
  "v_exp_f32 v147, v147\n\t" \
  "v_exp_f32 v148, v148\n\t" \
  "v_exp_f32 v149, v149\n\t" \
  "v_exp_f32 v150, v150\n\t" \
  "v_exp_f32 v151, v151\n\t" \
  "v_mfma_f32_32x32x16_bf16 v[96:111], v[116:119], %7, %9\n\t" \
  "ds_read_b128 v[116:119], %2 offset:25344\n\t" \
  "v_exp_f32 v152, v152\n\t" \
  "v_exp_f32 v153, v153\n\t" \
  "v_exp_f32 v154, v154\n\t" \
  "v_smfmac_f32_32x32x32_bf16 %1, v[120:123], v[136:143], %10\n\t" \
  "ds_read_b128 v[120:123], %3 offset:23232\n\t" \
  "v_exp_f32 v155, v155\n\t" \
  "v_exp_f32 v156, v156\n\t" \
  "v_smfmac_f32_32x32x32_bf16 %1, v[128:131], v[144:151], %10\n\t" \
  "ds_read_b128 v[128:131], %3 offset:23776\n\t" \
  "v_exp_f32 v157, v157\n\t" \
  "v_exp_f32 v158, v158\n\t" \
  "v_exp_f32 v159, v159\n\t" \
  "v_exp_f32 v160, v160\n\t" \
  "v_exp_f32 v161, v161\n\t" \
  "v_exp_f32 v162, v162\n\t" \
  "v_exp_f32 v163, v163\n\t" \
  "v_exp_f32 v164, v164\n\t" \
  "v_exp_f32 v165, v165\n\t" \
  "v_exp_f32 v166, v166\n\t" \
  "v_exp_f32 v167, v167\n\t" \
  "v_mfma_f32_32x32x16_bf16 v[136:151], v[112:115], %6, %8\n\t" \
  "v_exp_f32 v96, v96\n\t" \
  "v_exp_f32 v97, v97\n\t" \
  "v_exp_f32 v98, v98\n\t" \
  "v_exp_f32 v99, v99\n\t" \
  "v_smfmac_f32_32x32x32_bf16 %0, v[124:127], v[152:159], %10\n\t" \
  "v_exp_f32 v100, v100\n\t" \
  "v_exp_f32 v101, v101\n\t" \
  "v_smfmac_f32_32x32x32_bf16 %0, v[132:135], v[160:167], %10\n\t" \
  "s_waitcnt lgkmcnt(0)\n\t" \
  "v_exp_f32 v102, v102\n\t" \
  "v_exp_f32 v103, v103\n\t" \
  "v_exp_f32 v104, v104\n\t" \
  "v_exp_f32 v105, v105\n\t" \
  "v_exp_f32 v106, v106\n\t" \
  "v_exp_f32 v107, v107\n\t" \
  "v_exp_f32 v108, v108\n\t" \
  "v_exp_f32 v109, v109\n\t" \
  "v_exp_f32 v110, v110\n\t" \
  "v_exp_f32 v111, v111\n\t" \
  "v_mfma_f32_32x32x16_bf16 v[152:167], v[112:115], %7, %9\n\t" \
  "ds_read_b128 v[112:115], %2 offset:27456\n\t" \
  "v_exp_f32 v136, v136\n\t" \
  "v_exp_f32 v137, v137\n\t" \
  "v_exp_f32 v138, v138\n\t" \
  "v_smfmac_f32_32x32x32_bf16 %1, v[124:127], v[96:103], %10\n\t" \
  "ds_read_b128 v[124:127], %3 offset:25344\n\t" \
  "v_exp_f32 v139, v139\n\t" \
  "v_exp_f32 v140, v140\n\t" \
  "v_smfmac_f32_32x32x32_bf16 %1, v[132:135], v[104:111], %10\n\t" \
  "ds_read_b128 v[132:135], %3 offset:25888\n\t" \
  "v_exp_f32 v141, v141\n\t" \
  "v_exp_f32 v142, v142\n\t" \
  "v_exp_f32 v143, v143\n\t" \
  "v_exp_f32 v144, v144\n\t" \
  "v_exp_f32 v145, v145\n\t" \
  "v_exp_f32 v146, v146\n\t" \
  "v_exp_f32 v147, v147\n\t" \
  "v_exp_f32 v148, v148\n\t" \
  "v_exp_f32 v149, v149\n\t" \
  "v_exp_f32 v150, v150\n\t" \
  "v_exp_f32 v151, v151\n\t" \
  "v_mfma_f32_32x32x16_bf16 v[96:111], v[116:119], %6, %8\n\t" \
  "v_exp_f32 v152, v152\n\t" \
  "v_exp_f32 v153, v153\n\t" \
  "v_exp_f32 v154, v154\n\t" \
  "v_exp_f32 v155, v155\n\t" \
  "v_smfmac_f32_32x32x32_bf16 %0, v[120:123], v[136:143], %10\n\t" \
  "v_exp_f32 v156, v156\n\t" \
  "v_exp_f32 v157, v157\n\t" \
  "v_smfmac_f32_32x32x32_bf16 %0, v[128:131], v[144:151], %10\n\t" \
  "s_waitcnt lgkmcnt(0)\n\t" \
  "v_exp_f32 v158, v158\n\t" \
  "v_exp_f32 v159, v159\n\t" \
  "v_exp_f32 v160, v160\n\t" \
  "v_exp_f32 v161, v161\n\t" \
  "v_exp_f32 v162, v162\n\t" \
  "v_exp_f32 v163, v163\n\t" \
  "v_exp_f32 v164, v164\n\t" \
  "v_exp_f32 v165, v165\n\t" \
  "v_exp_f32 v166, v166\n\t" \
  "v_exp_f32 v167, v167\n\t" \
  "v_mfma_f32_32x32x16_bf16 v[136:151], v[116:119], %7, %9\n\t" \
  "ds_read_b128 v[116:119], %2 offset:29568\n\t" \
  "v_exp_f32 v96, v96\n\t" \
  "v_exp_f32 v97, v97\n\t" \
  "v_exp_f32 v98, v98\n\t" \
  "v_smfmac_f32_32x32x32_bf16 %1, v[120:123], v[152:159], %10\n\t" \
  "ds_read_b128 v[120:123], %3 offset:27456\n\t" \
  "v_exp_f32 v99, v99\n\t" \
  "v_exp_f32 v100, v100\n\t" \
  "v_smfmac_f32_32x32x32_bf16 %1, v[128:131], v[160:167], %10\n\t" \
  "ds_read_b128 v[128:131], %3 offset:28000\n\t" \
  "v_exp_f32 v101, v101\n\t" \
  "v_exp_f32 v102, v102\n\t" \
  "v_exp_f32 v103, v103\n\t" \
  "v_exp_f32 v104, v104\n\t" \
  "v_exp_f32 v105, v105\n\t" \
  "v_exp_f32 v106, v106\n\t" \
  "v_exp_f32 v107, v107\n\t" \
  "v_exp_f32 v108, v108\n\t" \
  "v_exp_f32 v109, v109\n\t" \
  "v_exp_f32 v110, v110\n\t" \
  "v_exp_f32 v111, v111\n\t" \
  "v_mfma_f32_32x32x16_bf16 v[152:167], v[112:115], %6, %8\n\t" \
  "v_exp_f32 v136, v136\n\t" \
  "v_exp_f32 v137, v137\n\t" \
  "v_exp_f32 v138, v138\n\t" \
  "v_exp_f32 v139, v139\n\t" \
  "v_smfmac_f32_32x32x32_bf16 %0, v[124:127], v[96:103], %10\n\t" \
  "v_exp_f32 v140, v140\n\t" \
  "v_exp_f32 v141, v141\n\t" \
  "v_smfmac_f32_32x32x32_bf16 %0, v[132:135], v[104:111], %10\n\t" \
  "s_waitcnt lgkmcnt(0)\n\t" \
  "v_exp_f32 v142, v142\n\t" \
  "v_exp_f32 v143, v143\n\t" \
  "v_exp_f32 v144, v144\n\t" \
  "v_exp_f32 v145, v145\n\t" \
  "v_exp_f32 v146, v146\n\t" \
  "v_exp_f32 v147, v147\n\t" \
  "v_exp_f32 v148, v148\n\t" \
  "v_exp_f32 v149, v149\n\t" \
  "v_exp_f32 v150, v150\n\t" \
  "v_exp_f32 v151, v151\n\t" \
  "v_mfma_f32_32x32x16_bf16 v[96:111], v[112:115], %7, %9\n\t" \
  "ds_read_b128 v[112:115], %2 offset:31680\n\t" \
  "v_exp_f32 v152, v152\n\t" \
  "v_exp_f32 v153, v153\n\t" \
  "v_exp_f32 v154, v154\n\t" \
  "v_smfmac_f32_32x32x32_bf16 %1, v[124:127], v[136:143], %10\n\t" \
  "ds_read_b128 v[124:127], %3 offset:29568\n\t" \
  "v_exp_f32 v155, v155\n\t" \
  "v_exp_f32 v156, v156\n\t" \
  "v_smfmac_f32_32x32x32_bf16 %1, v[132:135], v[144:151], %10\n\t" \
  "ds_read_b128 v[132:135], %3 offset:30112\n\t" \
  "v_exp_f32 v157, v157\n\t" \
  "v_exp_f32 v158, v158\n\t" \
  "v_exp_f32 v159, v159\n\t" \
  "v_exp_f32 v160, v160\n\t" \
  "v_exp_f32 v161, v161\n\t" \
  "v_exp_f32 v162, v162\n\t" \
  "v_exp_f32 v163, v163\n\t" \
  "v_exp_f32 v164, v164\n\t" \
  "v_exp_f32 v165, v165\n\t" \
  "v_exp_f32 v166, v166\n\t" \
  "v_exp_f32 v167, v167\n\t" \
  "v_mfma_f32_32x32x16_bf16 v[136:151], v[116:119], %6, %8\n\t" \
  "v_exp_f32 v96, v96\n\t" \
  "v_exp_f32 v97, v97\n\t" \
  "v_exp_f32 v98, v98\n\t" \
  "v_exp_f32 v99, v99\n\t" \
  "v_smfmac_f32_32x32x32_bf16 %0, v[120:123], v[152:159], %10\n\t" \
  "v_exp_f32 v100, v100\n\t" \
  "v_exp_f32 v101, v101\n\t" \
  "v_smfmac_f32_32x32x32_bf16 %0, v[128:131], v[160:167], %10\n\t" \
  "s_waitcnt lgkmcnt(0)\n\t" \
  "v_exp_f32 v102, v102\n\t" \
  "v_exp_f32 v103, v103\n\t" \
  "v_exp_f32 v104, v104\n\t" \
  "v_exp_f32 v105, v105\n\t" \
  "v_exp_f32 v106, v106\n\t" \
  "v_exp_f32 v107, v107\n\t" \
  "v_exp_f32 v108, v108\n\t" \
  "v_exp_f32 v109, v109\n\t" \
  "v_exp_f32 v110, v110\n\t" \
  "v_exp_f32 v111, v111\n\t" \
  "v_mfma_f32_32x32x16_bf16 v[152:167], v[116:119], %7, %9\n\t" \
  "ds_read_b128 v[116:119], %2 offset:33792\n\t" \
  "v_exp_f32 v136, v136\n\t" \
  "v_exp_f32 v137, v137\n\t" \
  "v_exp_f32 v138, v138\n\t" \
  "v_smfmac_f32_32x32x32_bf16 %1, v[120:123], v[96:103], %10\n\t" \
  "ds_read_b128 v[120:123], %3 offset:31680\n\t" \
  "v_exp_f32 v139, v139\n\t" \
  "v_exp_f32 v140, v140\n\t" \
  "v_smfmac_f32_32x32x32_bf16 %1, v[128:131], v[104:111], %10\n\t" \
  "ds_read_b128 v[128:131], %3 offset:32224\n\t" \
  "v_exp_f32 v141, v141\n\t" \
  "v_exp_f32 v142, v142\n\t" \
  "v_exp_f32 v143, v143\n\t" \
  "v_exp_f32 v144, v144\n\t" \
  "v_exp_f32 v145, v145\n\t" \
  "v_exp_f32 v146, v146\n\t" \
  "v_exp_f32 v147, v147\n\t" \
  "v_exp_f32 v148, v148\n\t" \
  "v_exp_f32 v149, v149\n\t" \
  "v_exp_f32 v150, v150\n\t" \
  "v_exp_f32 v151, v151\n\t" \
  "v_mfma_f32_32x32x16_bf16 v[96:111], v[112:115], %6, %8\n\t" \
  "v_exp_f32 v152, v152\n\t" \
  "v_exp_f32 v153, v153\n\t" \
  "v_exp_f32 v154, v154\n\t" \
  "v_exp_f32 v155, v155\n\t" \
  "v_smfmac_f32_32x32x32_bf16 %0, v[124:127], v[136:143], %10\n\t" \
  "v_exp_f32 v156, v156\n\t" \
  "v_exp_f32 v157, v157\n\t" \
  "v_smfmac_f32_32x32x32_bf16 %0, v[132:135], v[144:151], %10\n\t" \
  "s_waitcnt lgkmcnt(0)\n\t" \
  "v_exp_f32 v158, v158\n\t" \
  "v_exp_f32 v159, v159\n\t" \
  "v_exp_f32 v160, v160\n\t" \
  "v_exp_f32 v161, v161\n\t" \
  "v_exp_f32 v162, v162\n\t" \
  "v_exp_f32 v163, v163\n\t" \
  "v_exp_f32 v164, v164\n\t" \
  "v_exp_f32 v165, v165\n\t" \
  "v_exp_f32 v166, v166\n\t" \
  "v_exp_f32 v167, v167\n\t" \
  "v_mfma_f32_32x32x16_bf16 v[136:151], v[112:115], %7, %9\n\t" \
  "ds_read_b128 v[112:115], %2 offset:35904\n\t" \
  "v_exp_f32 v96, v96\n\t" \
  "v_exp_f32 v97, v97\n\t" \
  "v_exp_f32 v98, v98\n\t" \
  "v_smfmac_f32_32x32x32_bf16 %1, v[124:127], v[152:159], %10\n\t" \
  "ds_read_b128 v[124:127], %3 offset:33792\n\t" \
  "v_exp_f32 v99, v99\n\t" \
  "v_exp_f32 v100, v100\n\t" \
  "v_smfmac_f32_32x32x32_bf16 %1, v[132:135], v[160:167], %10\n\t" \
  "ds_read_b128 v[132:135], %3 offset:34336\n\t" \
  "v_exp_f32 v101, v101\n\t" \
  "v_exp_f32 v102, v102\n\t" \
  "v_exp_f32 v103, v103\n\t" \
  "v_exp_f32 v104, v104\n\t" \
  "v_exp_f32 v105, v105\n\t" \
  "v_exp_f32 v106, v106\n\t" \
  "v_exp_f32 v107, v107\n\t" \
  "v_exp_f32 v108, v108\n\t" \
  "v_exp_f32 v109, v109\n\t" \
  "v_exp_f32 v110, v110\n\t" \
  "v_exp_f32 v111, v111\n\t" \
  "v_mfma_f32_32x32x16_bf16 v[152:167], v[116:119], %6, %8\n\t" \
  "v_exp_f32 v136, v136\n\t" \
  "v_exp_f32 v137, v137\n\t" \
  "v_exp_f32 v138, v138\n\t" \
  "v_exp_f32 v139, v139\n\t" \
  "v_smfmac_f32_32x32x32_bf16 %0, v[120:123], v[96:103], %10\n\t" \
  "v_exp_f32 v140, v140\n\t" \
  "v_exp_f32 v141, v141\n\t" \
  "v_smfmac_f32_32x32x32_bf16 %0, v[128:131], v[104:111], %10\n\t" \
  "s_waitcnt lgkmcnt(0)\n\t" \
  "v_exp_f32 v142, v142\n\t" \
  "v_exp_f32 v143, v143\n\t" \
  "v_exp_f32 v144, v144\n\t" \
  "v_exp_f32 v145, v145\n\t" \
  "v_exp_f32 v146, v146\n\t" \
  "v_exp_f32 v147, v147\n\t" \
  "v_exp_f32 v148, v148\n\t" \
  "v_exp_f32 v149, v149\n\t" \
  "v_exp_f32 v150, v150\n\t" \
  "v_exp_f32 v151, v151\n\t" \
  "v_mfma_f32_32x32x16_bf16 v[96:111], v[116:119], %7, %9\n\t" \
  "ds_read_b128 v[116:119], %2 offset:38016\n\t" \
  "v_exp_f32 v152, v152\n\t" \
  "v_exp_f32 v153, v153\n\t" \
  "v_exp_f32 v154, v154\n\t" \
  "v_smfmac_f32_32x32x32_bf16 %1, v[120:123], v[136:143], %10\n\t" \
  "ds_read_b128 v[120:123], %3 offset:35904\n\t" \
  "v_exp_f32 v155, v155\n\t" \
  "v_exp_f32 v156, v156\n\t" \
  "v_smfmac_f32_32x32x32_bf16 %1, v[128:131], v[144:151], %10\n\t" \
  "ds_read_b128 v[128:131], %3 offset:36448\n\t" \
  "v_exp_f32 v157, v157\n\t" \
  "v_exp_f32 v158, v158\n\t" \
  "v_exp_f32 v159, v159\n\t" \
  "v_exp_f32 v160, v160\n\t" \
  "v_exp_f32 v161, v161\n\t" \
  "v_exp_f32 v162, v162\n\t" \
  "v_exp_f32 v163, v163\n\t" \
  "v_exp_f32 v164, v164\n\t" \
  "v_exp_f32 v165, v165\n\t" \
  "v_exp_f32 v166, v166\n\t" \
  "v_exp_f32 v167, v167\n\t" \
  "v_mfma_f32_32x32x16_bf16 v[136:151], v[112:115], %6, %8\n\t" \
  "v_exp_f32 v96, v96\n\t" \
  "v_exp_f32 v97, v97\n\t" \
  "v_exp_f32 v98, v98\n\t" \
  "v_exp_f32 v99, v99\n\t" \
  "v_smfmac_f32_32x32x32_bf16 %0, v[124:127], v[152:159], %10\n\t" \
  "v_exp_f32 v100, v100\n\t" \
  "v_exp_f32 v101, v101\n\t" \
  "v_smfmac_f32_32x32x32_bf16 %0, v[132:135], v[160:167], %10\n\t" \
  "s_waitcnt lgkmcnt(0)\n\t" \
  "v_exp_f32 v102, v102\n\t" \
  "v_exp_f32 v103, v103\n\t" \
  "v_exp_f32 v104, v104\n\t" \
  "v_exp_f32 v105, v105\n\t" \
  "v_exp_f32 v106, v106\n\t" \
  "v_exp_f32 v107, v107\n\t" \
  "v_exp_f32 v108, v108\n\t" \
  "v_exp_f32 v109, v109\n\t" \
  "v_exp_f32 v110, v110\n\t" \
  "v_exp_f32 v111, v111\n\t" \
  "v_mfma_f32_32x32x16_bf16 v[152:167], v[112:115], %7, %9\n\t" \
  "ds_read_b128 v[112:115], %2 offset:40128\n\t" \
  "v_exp_f32 v136, v136\n\t" \
  "v_exp_f32 v137, v137\n\t" \
  "v_exp_f32 v138, v138\n\t" \
  "v_smfmac_f32_32x32x32_bf16 %1, v[124:127], v[96:103], %10\n\t" \
  "ds_read_b128 v[124:127], %3 offset:38016\n\t" \
  "v_exp_f32 v139, v139\n\t" \
  "v_exp_f32 v140, v140\n\t" \
  "v_smfmac_f32_32x32x32_bf16 %1, v[132:135], v[104:111], %10\n\t" \
  "ds_read_b128 v[132:135], %3 offset:38560\n\t" \
  "v_exp_f32 v141, v141\n\t" \
  "v_exp_f32 v142, v142\n\t" \
  "v_exp_f32 v143, v143\n\t" \
  "v_exp_f32 v144, v144\n\t" \
  "v_exp_f32 v145, v145\n\t" \
  "v_exp_f32 v146, v146\n\t" \
  "v_exp_f32 v147, v147\n\t" \
  "v_exp_f32 v148, v148\n\t" \
  "v_exp_f32 v149, v149\n\t" \
  "v_exp_f32 v150, v150\n\t" \
  "v_exp_f32 v151, v151\n\t" \
  "v_mfma_f32_32x32x16_bf16 v[96:111], v[116:119], %6, %8\n\t" \
  "v_exp_f32 v152, v152\n\t" \
  "v_exp_f32 v153, v153\n\t" \
  "v_exp_f32 v154, v154\n\t" \
  "v_exp_f32 v155, v155\n\t" \
  "v_smfmac_f32_32x32x32_bf16 %0, v[120:123], v[136:143], %10\n\t" \
  "v_exp_f32 v156, v156\n\t" \
  "v_exp_f32 v157, v157\n\t" \
  "v_smfmac_f32_32x32x32_bf16 %0, v[128:131], v[144:151], %10\n\t" \
  "s_waitcnt lgkmcnt(0)\n\t" \
  "v_exp_f32 v158, v158\n\t" \
  "v_exp_f32 v159, v159\n\t" \
  "v_exp_f32 v160, v160\n\t" \
  "v_exp_f32 v161, v161\n\t" \
  "v_exp_f32 v162, v162\n\t" \
  "v_exp_f32 v163, v163\n\t" \
  "v_exp_f32 v164, v164\n\t" \
  "v_exp_f32 v165, v165\n\t" \
  "v_exp_f32 v166, v166\n\t" \
  "v_exp_f32 v167, v167\n\t" \
  "v_mfma_f32_32x32x16_bf16 v[136:151], v[116:119], %7, %9\n\t" \
  "ds_read_b128 v[116:119], %2 offset:42240\n\t" \
  "v_exp_f32 v96, v96\n\t" \
  "v_exp_f32 v97, v97\n\t" \
  "v_exp_f32 v98, v98\n\t" \
  "v_smfmac_f32_32x32x32_bf16 %1, v[120:123], v[152:159], %10\n\t" \
  "ds_read_b128 v[120:123], %3 offset:40128\n\t" \
  "v_exp_f32 v99, v99\n\t" \
  "v_exp_f32 v100, v100\n\t" \
  "v_smfmac_f32_32x32x32_bf16 %1, v[128:131], v[160:167], %10\n\t" \
  "ds_read_b128 v[128:131], %3 offset:40672\n\t" \
  "v_exp_f32 v101, v101\n\t" \
  "v_exp_f32 v102, v102\n\t" \
  "v_exp_f32 v103, v103\n\t" \
  "v_exp_f32 v104, v104\n\t" \
  "v_exp_f32 v105, v105\n\t" \
  "v_exp_f32 v106, v106\n\t" \
  "v_exp_f32 v107, v107\n\t" \
  "v_exp_f32 v108, v108\n\t" \
  "v_exp_f32 v109, v109\n\t" \
  "v_exp_f32 v110, v110\n\t" \
  "v_exp_f32 v111, v111\n\t" \
  "v_mfma_f32_32x32x16_bf16 v[152:167], v[112:115], %6, %8\n\t" \
  "v_exp_f32 v136, v136\n\t" \
  "v_exp_f32 v137, v137\n\t" \
  "v_exp_f32 v138, v138\n\t" \
  "v_exp_f32 v139, v139\n\t" \
  "v_smfmac_f32_32x32x32_bf16 %0, v[124:127], v[96:103], %10\n\t" \
  "v_exp_f32 v140, v140\n\t" \
  "v_exp_f32 v141, v141\n\t" \
  "v_smfmac_f32_32x32x32_bf16 %0, v[132:135], v[104:111], %10\n\t" \
  "s_waitcnt lgkmcnt(0)\n\t" \
  "v_exp_f32 v142, v142\n\t" \
  "v_exp_f32 v143, v143\n\t" \
  "v_exp_f32 v144, v144\n\t" \
  "v_exp_f32 v145, v145\n\t" \
  "v_exp_f32 v146, v146\n\t" \
  "v_exp_f32 v147, v147\n\t" \
  "v_exp_f32 v148, v148\n\t" \
  "v_exp_f32 v149, v149\n\t" \
  "v_exp_f32 v150, v150\n\t" \
  "v_exp_f32 v151, v151\n\t" \
  "v_mfma_f32_32x32x16_bf16 v[96:111], v[112:115], %7, %9\n\t" \
  "ds_read_b128 v[112:115], %2 offset:44352\n\t" \
  "v_exp_f32 v152, v152\n\t" \
  "v_exp_f32 v153, v153\n\t" \
  "v_exp_f32 v154, v154\n\t" \
  "v_smfmac_f32_32x32x32_bf16 %1, v[124:127], v[136:143], %10\n\t" \
  "ds_read_b128 v[124:127], %3 offset:42240\n\t" \
  "v_exp_f32 v155, v155\n\t" \
  "v_exp_f32 v156, v156\n\t" \
  "v_smfmac_f32_32x32x32_bf16 %1, v[132:135], v[144:151], %10\n\t" \
  "ds_read_b128 v[132:135], %3 offset:42784\n\t" \
  "v_exp_f32 v157, v157\n\t" \
  "v_exp_f32 v158, v158\n\t" \
  "v_exp_f32 v159, v159\n\t" \
  "v_exp_f32 v160, v160\n\t" \
  "v_exp_f32 v161, v161\n\t" \
  "v_exp_f32 v162, v162\n\t" \
  "v_exp_f32 v163, v163\n\t" \
  "v_exp_f32 v164, v164\n\t" \
  "v_exp_f32 v165, v165\n\t" \
  "v_exp_f32 v166, v166\n\t" \
  "v_exp_f32 v167, v167\n\t" \
  "v_mfma_f32_32x32x16_bf16 v[136:151], v[116:119], %6, %8\n\t" \
  "v_exp_f32 v96, v96\n\t" \
  "v_exp_f32 v97, v97\n\t" \
  "v_exp_f32 v98, v98\n\t" \
  "v_exp_f32 v99, v99\n\t" \
  "v_smfmac_f32_32x32x32_bf16 %0, v[120:123], v[152:159], %10\n\t" \
  "v_exp_f32 v100, v100\n\t" \
  "v_exp_f32 v101, v101\n\t" \
  "v_smfmac_f32_32x32x32_bf16 %0, v[128:131], v[160:167], %10\n\t" \
  "s_waitcnt lgkmcnt(0)\n\t" \
  "v_exp_f32 v102, v102\n\t" \
  "v_exp_f32 v103, v103\n\t" \
  "v_exp_f32 v104, v104\n\t" \
  "v_exp_f32 v105, v105\n\t" \
  "v_exp_f32 v106, v106\n\t" \
  "v_exp_f32 v107, v107\n\t" \
  "v_exp_f32 v108, v108\n\t" \
  "v_exp_f32 v109, v109\n\t" \
  "v_exp_f32 v110, v110\n\t" \
  "v_exp_f32 v111, v111\n\t" \
  "v_mfma_f32_32x32x16_bf16 v[152:167], v[116:119], %7, %9\n\t" \
  "ds_read_b128 v[116:119], %2 offset:46464\n\t" \
  "v_exp_f32 v136, v136\n\t" \
  "v_exp_f32 v137, v137\n\t" \
  "v_exp_f32 v138, v138\n\t" \
  "v_smfmac_f32_32x32x32_bf16 %1, v[120:123], v[96:103], %10\n\t" \
  "ds_read_b128 v[120:123], %3 offset:44352\n\t" \
  "v_exp_f32 v139, v139\n\t" \
  "v_exp_f32 v140, v140\n\t" \
  "v_smfmac_f32_32x32x32_bf16 %1, v[128:131], v[104:111], %10\n\t" \
  "ds_read_b128 v[128:131], %3 offset:44896\n\t" \
  "v_exp_f32 v141, v141\n\t" \
  "v_exp_f32 v142, v142\n\t" \
  "v_exp_f32 v143, v143\n\t" \
  "v_exp_f32 v144, v144\n\t" \
  "v_exp_f32 v145, v145\n\t" \
  "v_exp_f32 v146, v146\n\t" \
  "v_exp_f32 v147, v147\n\t" \
  "v_exp_f32 v148, v148\n\t" \
  "v_exp_f32 v149, v149\n\t" \
  "v_exp_f32 v150, v150\n\t" \
  "v_exp_f32 v151, v151\n\t" \
  "v_mfma_f32_32x32x16_bf16 v[96:111], v[112:115], %6, %8\n\t" \
  "v_exp_f32 v152, v152\n\t" \
  "v_exp_f32 v153, v153\n\t" \
  "v_exp_f32 v154, v154\n\t" \
  "v_exp_f32 v155, v155\n\t" \
  "v_smfmac_f32_32x32x32_bf16 %0, v[124:127], v[136:143], %10\n\t" \
  "v_exp_f32 v156, v156\n\t" \
  "v_exp_f32 v157, v157\n\t" \
  "v_smfmac_f32_32x32x32_bf16 %0, v[132:135], v[144:151], %10\n\t" \
  "s_waitcnt lgkmcnt(0)\n\t" \
  "v_exp_f32 v158, v158\n\t" \
  "v_exp_f32 v159, v159\n\t" \
  "v_exp_f32 v160, v160\n\t" \
  "v_exp_f32 v161, v161\n\t" \
  "v_exp_f32 v162, v162\n\t" \
  "v_exp_f32 v163, v163\n\t" \
  "v_exp_f32 v164, v164\n\t" \
  "v_exp_f32 v165, v165\n\t" \
  "v_exp_f32 v166, v166\n\t" \
  "v_exp_f32 v167, v167\n\t" \
  "v_mfma_f32_32x32x16_bf16 v[136:151], v[112:115], %7, %9\n\t" \
  "ds_read_b128 v[112:115], %2 offset:48576\n\t" \
  "v_exp_f32 v96, v96\n\t" \
  "v_exp_f32 v97, v97\n\t" \
  "v_exp_f32 v98, v98\n\t" \
  "v_smfmac_f32_32x32x32_bf16 %1, v[124:127], v[152:159], %10\n\t" \
  "ds_read_b128 v[124:127], %3 offset:46464\n\t" \
  "v_exp_f32 v99, v99\n\t" \
  "v_exp_f32 v100, v100\n\t" \
  "v_smfmac_f32_32x32x32_bf16 %1, v[132:135], v[160:167], %10\n\t" \
  "ds_read_b128 v[132:135], %3 offset:47008\n\t" \
  "v_exp_f32 v101, v101\n\t" \
  "v_exp_f32 v102, v102\n\t" \
  "v_exp_f32 v103, v103\n\t" \
  "v_exp_f32 v104, v104\n\t" \
  "v_exp_f32 v105, v105\n\t" \
  "v_exp_f32 v106, v106\n\t" \
  "v_exp_f32 v107, v107\n\t" \
  "v_exp_f32 v108, v108\n\t" \
  "v_exp_f32 v109, v109\n\t" \
  "v_exp_f32 v110, v110\n\t" \
  "v_exp_f32 v111, v111\n\t" \
  "v_mfma_f32_32x32x16_bf16 v[152:167], v[116:119], %6, %8\n\t" \
  "ds_read_b32 %4, %11 offset:12\n\t" \
  "v_exp_f32 v136, v136\n\t" \
  "v_exp_f32 v137, v137\n\t" \
  "v_exp_f32 v138, v138\n\t" \
  "v_exp_f32 v139, v139\n\t" \
  "v_smfmac_f32_32x32x32_bf16 %0, v[120:123], v[96:103], %10\n\t" \
  "v_exp_f32 v140, v140\n\t" \
  "v_exp_f32 v141, v141\n\t" \
  "v_smfmac_f32_32x32x32_bf16 %0, v[128:131], v[104:111], %10\n\t" \
  "s_waitcnt lgkmcnt(0)\n\t" \
  "v_exp_f32 v142, v142\n\t" \
  "v_exp_f32 v143, v143\n\t" \
  "v_exp_f32 v144, v144\n\t" \
  "v_exp_f32 v145, v145\n\t" \
  "v_exp_f32 v146, v146\n\t" \
  "v_exp_f32 v147, v147\n\t" \
  "v_exp_f32 v148, v148\n\t" \
  "v_exp_f32 v149, v149\n\t" \
  "v_exp_f32 v150, v150\n\t" \
  "v_exp_f32 v151, v151\n\t" \
  "v_mfma_f32_32x32x16_bf16 v[96:111], v[116:119], %7, %9\n\t" \
  "v_readfirstlane_b32 %5, %4\n\t" \
  "s_nop 0\n\t" \
  "s_cmp_lt_u32 %5, 8\n\t" \
  "s_cbranch_scc1 13f\n\t" \
  "23:\n\t" \
  "ds_read_b128 v[116:119], %2 offset:50688\n\t" \
  "v_exp_f32 v152, v152\n\t" \
  "v_exp_f32 v153, v153\n\t" \
  "v_exp_f32 v154, v154\n\t" \
  "v_smfmac_f32_32x32x32_bf16 %1, v[120:123], v[136:143], %10\n\t" \
  "ds_read_b128 v[120:123], %3 offset:48576\n\t" \
  "v_exp_f32 v155, v155\n\t" \
  "v_exp_f32 v156, v156\n\t" \
  "v_smfmac_f32_32x32x32_bf16 %1, v[128:131], v[144:151], %10\n\t" \
  "ds_read_b128 v[128:131], %3 offset:49120\n\t" \
  "v_exp_f32 v157, v157\n\t" \
  "v_exp_f32 v158, v158\n\t" \
  "v_exp_f32 v159, v159\n\t" \
  "v_exp_f32 v160, v160\n\t" \
  "v_exp_f32 v161, v161\n\t" \
  "v_exp_f32 v162, v162\n\t" \
  "v_exp_f32 v163, v163\n\t" \
  "v_exp_f32 v164, v164\n\t" \
  "v_exp_f32 v165, v165\n\t" \
  "v_exp_f32 v166, v166\n\t" \
  "v_exp_f32 v167, v167\n\t" \
  "v_mfma_f32_32x32x16_bf16 v[136:151], v[112:115], %6, %8\n\t" \
  "v_exp_f32 v96, v96\n\t" \
  "v_exp_f32 v97, v97\n\t" \
  "v_exp_f32 v98, v98\n\t" \
  "v_exp_f32 v99, v99\n\t" \
  "v_smfmac_f32_32x32x32_bf16 %0, v[124:127], v[152:159], %10\n\t" \
  "v_exp_f32 v100, v100\n\t" \
  "v_exp_f32 v101, v101\n\t" \
  "v_smfmac_f32_32x32x32_bf16 %0, v[132:135], v[160:167], %10\n\t" \
  "s_waitcnt lgkmcnt(0)\n\t" \
  "v_exp_f32 v102, v102\n\t" \
  "v_exp_f32 v103, v103\n\t" \
  "v_exp_f32 v104, v104\n\t" \
  "v_exp_f32 v105, v105\n\t" \
  "v_exp_f32 v106, v106\n\t" \
  "v_exp_f32 v107, v107\n\t" \
  "v_exp_f32 v108, v108\n\t" \
  "v_exp_f32 v109, v109\n\t" \
  "v_exp_f32 v110, v110\n\t" \
  "v_exp_f32 v111, v111\n\t" \
  "v_mfma_f32_32x32x16_bf16 v[152:167], v[112:115], %7, %9\n\t" \
  "ds_read_b128 v[112:115], %2 offset:52800\n\t" \
  "v_exp_f32 v136, v136\n\t" \
  "v_exp_f32 v137, v137\n\t" \
  "v_exp_f32 v138, v138\n\t" \
  "v_smfmac_f32_32x32x32_bf16 %1, v[124:127], v[96:103], %10\n\t" \
  "ds_read_b128 v[124:127], %3 offset:50688\n\t" \
  "v_exp_f32 v139, v139\n\t" \
  "v_exp_f32 v140, v140\n\t" \
  "v_smfmac_f32_32x32x32_bf16 %1, v[132:135], v[104:111], %10\n\t" \
  "ds_read_b128 v[132:135], %3 offset:51232\n\t" \
  "v_exp_f32 v141, v141\n\t" \
  "v_exp_f32 v142, v142\n\t" \
  "v_exp_f32 v143, v143\n\t" \
  "v_exp_f32 v144, v144\n\t" \
  "v_exp_f32 v145, v145\n\t" \
  "v_exp_f32 v146, v146\n\t" \
  "v_exp_f32 v147, v147\n\t" \
  "v_exp_f32 v148, v148\n\t" \
  "v_exp_f32 v149, v149\n\t" \
  "v_exp_f32 v150, v150\n\t" \
  "v_exp_f32 v151, v151\n\t" \
  "v_mfma_f32_32x32x16_bf16 v[96:111], v[116:119], %6, %8\n\t" \
  "v_exp_f32 v152, v152\n\t" \
  "v_exp_f32 v153, v153\n\t" \
  "v_exp_f32 v154, v154\n\t" \
  "v_exp_f32 v155, v155\n\t" \
  "v_smfmac_f32_32x32x32_bf16 %0, v[120:123], v[136:143], %10\n\t" \
  "v_exp_f32 v156, v156\n\t" \
  "v_exp_f32 v157, v157\n\t" \
  "v_smfmac_f32_32x32x32_bf16 %0, v[128:131], v[144:151], %10\n\t" \
  "s_waitcnt lgkmcnt(0)\n\t" \
  "v_exp_f32 v158, v158\n\t" \
  "v_exp_f32 v159, v159\n\t" \
  "v_exp_f32 v160, v160\n\t" \
  "v_exp_f32 v161, v161\n\t" \
  "v_exp_f32 v162, v162\n\t" \
  "v_exp_f32 v163, v163\n\t" \
  "v_exp_f32 v164, v164\n\t" \
  "v_exp_f32 v165, v165\n\t" \
  "v_exp_f32 v166, v166\n\t" \
  "v_exp_f32 v167, v167\n\t" \
  "v_mfma_f32_32x32x16_bf16 v[136:151], v[116:119], %7, %9\n\t" \
  "ds_read_b128 v[116:119], %2 offset:54912\n\t" \
  "v_exp_f32 v96, v96\n\t" \
  "v_exp_f32 v97, v97\n\t" \
  "v_exp_f32 v98, v98\n\t" \
  "v_smfmac_f32_32x32x32_bf16 %1, v[120:123], v[152:159], %10\n\t" \
  "ds_read_b128 v[120:123], %3 offset:52800\n\t" \
  "v_exp_f32 v99, v99\n\t" \
  "v_exp_f32 v100, v100\n\t" \
  "v_smfmac_f32_32x32x32_bf16 %1, v[128:131], v[160:167], %10\n\t" \
  "ds_read_b128 v[128:131], %3 offset:53344\n\t" \
  "v_exp_f32 v101, v101\n\t" \
  "v_exp_f32 v102, v102\n\t" \
  "v_exp_f32 v103, v103\n\t" \
  "v_exp_f32 v104, v104\n\t" \
  "v_exp_f32 v105, v105\n\t" \
  "v_exp_f32 v106, v106\n\t" \
  "v_exp_f32 v107, v107\n\t" \
  "v_exp_f32 v108, v108\n\t" \
  "v_exp_f32 v109, v109\n\t" \
  "v_exp_f32 v110, v110\n\t" \
  "v_exp_f32 v111, v111\n\t" \
  "v_mfma_f32_32x32x16_bf16 v[152:167], v[112:115], %6, %8\n\t" \
  "v_mov_b32 %4, 1\n\t" \
  "s_mov_b64 exec, 1\n\t" \
  "ds_add_u32 %11, %4 offset:44\n\t" \
  "s_mov_b64 exec, -1\n\t" \
  "v_exp_f32 v136, v136\n\t" \
  "v_exp_f32 v137, v137\n\t" \
  "v_exp_f32 v138, v138\n\t" \
  "v_exp_f32 v139, v139\n\t" \
  "v_smfmac_f32_32x32x32_bf16 %0, v[124:127], v[96:103], %10\n\t" \
  "v_exp_f32 v140, v140\n\t" \
  "v_exp_f32 v141, v141\n\t" \
  "v_smfmac_f32_32x32x32_bf16 %0, v[132:135], v[104:111], %10\n\t" \
  "s_waitcnt lgkmcnt(0)\n\t" \
  "v_exp_f32 v142, v142\n\t" \
  "v_exp_f32 v143, v143\n\t" \
  "v_exp_f32 v144, v144\n\t" \
  "v_exp_f32 v145, v145\n\t" \
  "v_exp_f32 v146, v146\n\t" \
  "v_exp_f32 v147, v147\n\t" \
  "v_exp_f32 v148, v148\n\t" \
  "v_exp_f32 v149, v149\n\t" \
  "v_exp_f32 v150, v150\n\t" \
  "v_exp_f32 v151, v151\n\t" \
  "v_mfma_f32_32x32x16_bf16 v[96:111], v[112:115], %7, %9\n\t" \
  "ds_read_b128 v[112:115], %2 offset:57024\n\t" \
  "v_exp_f32 v152, v152\n\t" \
  "v_exp_f32 v153, v153\n\t" \
  "v_exp_f32 v154, v154\n\t" \
  "v_smfmac_f32_32x32x32_bf16 %1, v[124:127], v[136:143], %10\n\t" \
  "ds_read_b128 v[124:127], %3 offset:54912\n\t" \
  "v_exp_f32 v155, v155\n\t" \
  "v_exp_f32 v156, v156\n\t" \
  "v_smfmac_f32_32x32x32_bf16 %1, v[132:135], v[144:151], %10\n\t" \
  "ds_read_b128 v[132:135], %3 offset:55456\n\t" \
  "v_exp_f32 v157, v157\n\t" \
  "v_exp_f32 v158, v158\n\t" \
  "v_exp_f32 v159, v159\n\t" \
  "v_exp_f32 v160, v160\n\t" \
  "v_exp_f32 v161, v161\n\t" \
  "v_exp_f32 v162, v162\n\t" \
  "v_exp_f32 v163, v163\n\t" \
  "v_exp_f32 v164, v164\n\t" \
  "v_exp_f32 v165, v165\n\t" \
  "v_exp_f32 v166, v166\n\t" \
  "v_exp_f32 v167, v167\n\t" \
  "v_mfma_f32_32x32x16_bf16 v[136:151], v[116:119], %6, %8\n\t" \
  "v_exp_f32 v96, v96\n\t" \
  "v_exp_f32 v97, v97\n\t" \
  "v_exp_f32 v98, v98\n\t" \
  "v_exp_f32 v99, v99\n\t" \
  "v_smfmac_f32_32x32x32_bf16 %0, v[120:123], v[152:159], %10\n\t" \
  "v_exp_f32 v100, v100\n\t" \
  "v_exp_f32 v101, v101\n\t" \
  "v_smfmac_f32_32x32x32_bf16 %0, v[128:131], v[160:167], %10\n\t" \
  "s_waitcnt lgkmcnt(0)\n\t" \
  "v_exp_f32 v102, v102\n\t" \
  "v_exp_f32 v103, v103\n\t" \
  "v_exp_f32 v104, v104\n\t" \
  "v_exp_f32 v105, v105\n\t" \
  "v_exp_f32 v106, v106\n\t" \
  "v_exp_f32 v107, v107\n\t" \
  "v_exp_f32 v108, v108\n\t" \
  "v_exp_f32 v109, v109\n\t" \
  "v_exp_f32 v110, v110\n\t" \
  "v_exp_f32 v111, v111\n\t" \
  "v_mfma_f32_32x32x16_bf16 v[152:167], v[116:119], %7, %9\n\t" \
  "ds_read_b128 v[116:119], %2 offset:59136\n\t" \
  "v_exp_f32 v136, v136\n\t" \
  "v_exp_f32 v137, v137\n\t" \
  "v_exp_f32 v138, v138\n\t" \
  "v_smfmac_f32_32x32x32_bf16 %1, v[120:123], v[96:103], %10\n\t" \
  "ds_read_b128 v[120:123], %3 offset:57024\n\t" \
  "v_exp_f32 v139, v139\n\t" \
  "v_exp_f32 v140, v140\n\t" \
  "v_smfmac_f32_32x32x32_bf16 %1, v[128:131], v[104:111], %10\n\t" \
  "ds_read_b128 v[128:131], %3 offset:57568\n\t" \
  "v_exp_f32 v141, v141\n\t" \
  "v_exp_f32 v142, v142\n\t" \
  "v_exp_f32 v143, v143\n\t" \
  "v_exp_f32 v144, v144\n\t" \
  "v_exp_f32 v145, v145\n\t" \
  "v_exp_f32 v146, v146\n\t" \
  "v_exp_f32 v147, v147\n\t" \
  "v_exp_f32 v148, v148\n\t" \
  "v_exp_f32 v149, v149\n\t" \
  "v_exp_f32 v150, v150\n\t" \
  "v_exp_f32 v151, v151\n\t" \
  "v_mfma_f32_32x32x16_bf16 v[96:111], v[112:115], %6, %8\n\t" \
  "v_exp_f32 v152, v152\n\t" \
  "v_exp_f32 v153, v153\n\t" \
  "v_exp_f32 v154, v154\n\t" \
  "v_exp_f32 v155, v155\n\t" \
  "v_smfmac_f32_32x32x32_bf16 %0, v[124:127], v[136:143], %10\n\t" \
  "v_exp_f32 v156, v156\n\t" \
  "v_exp_f32 v157, v157\n\t" \
  "v_smfmac_f32_32x32x32_bf16 %0, v[132:135], v[144:151], %10\n\t" \
  "s_waitcnt lgkmcnt(0)\n\t" \
  "v_exp_f32 v158, v158\n\t" \
  "v_exp_f32 v159, v159\n\t" \
  "v_exp_f32 v160, v160\n\t" \
  "v_exp_f32 v161, v161\n\t" \
  "v_exp_f32 v162, v162\n\t" \
  "v_exp_f32 v163, v163\n\t" \
  "v_exp_f32 v164, v164\n\t" \
  "v_exp_f32 v165, v165\n\t" \
  "v_exp_f32 v166, v166\n\t" \
  "v_exp_f32 v167, v167\n\t" \
  "v_mfma_f32_32x32x16_bf16 v[136:151], v[112:115], %7, %9\n\t" \
  "ds_read_b128 v[112:115], %2 offset:61248\n\t" \
  "v_exp_f32 v96, v96\n\t" \
  "v_exp_f32 v97, v97\n\t" \
  "v_exp_f32 v98, v98\n\t" \
  "v_smfmac_f32_32x32x32_bf16 %1, v[124:127], v[152:159], %10\n\t" \
  "ds_read_b128 v[124:127], %3 offset:59136\n\t" \
  "v_exp_f32 v99, v99\n\t" \
  "v_exp_f32 v100, v100\n\t" \
  "v_smfmac_f32_32x32x32_bf16 %1, v[132:135], v[160:167], %10\n\t" \
  "ds_read_b128 v[132:135], %3 offset:59680\n\t" \
  "v_exp_f32 v101, v101\n\t" \
  "v_exp_f32 v102, v102\n\t" \
  "v_exp_f32 v103, v103\n\t" \
  "v_exp_f32 v104, v104\n\t" \
  "v_exp_f32 v105, v105\n\t" \
  "v_exp_f32 v106, v106\n\t" \
  "v_exp_f32 v107, v107\n\t" \
  "v_exp_f32 v108, v108\n\t" \
  "v_exp_f32 v109, v109\n\t" \
  "v_exp_f32 v110, v110\n\t" \
  "v_exp_f32 v111, v111\n\t" \
  "v_mfma_f32_32x32x16_bf16 v[152:167], v[116:119], %6, %8\n\t" \
  "v_exp_f32 v136, v136\n\t" \
  "v_exp_f32 v137, v137\n\t" \
  "v_exp_f32 v138, v138\n\t" \
  "v_exp_f32 v139, v139\n\t" \
  "v_smfmac_f32_32x32x32_bf16 %0, v[120:123], v[96:103], %10\n\t" \
  "v_exp_f32 v140, v140\n\t" \
  "v_exp_f32 v141, v141\n\t" \
  "v_smfmac_f32_32x32x32_bf16 %0, v[128:131], v[104:111], %10\n\t" \
  "s_waitcnt lgkmcnt(0)\n\t" \
  "v_exp_f32 v142, v142\n\t" \
  "v_exp_f32 v143, v143\n\t" \
  "v_exp_f32 v144, v144\n\t" \
  "v_exp_f32 v145, v145\n\t" \
  "v_exp_f32 v146, v146\n\t" \
  "v_exp_f32 v147, v147\n\t" \
  "v_exp_f32 v148, v148\n\t" \
  "v_exp_f32 v149, v149\n\t" \
  "v_exp_f32 v150, v150\n\t" \
  "v_exp_f32 v151, v151\n\t" \
  "v_mfma_f32_32x32x16_bf16 v[96:111], v[116:119], %7, %9\n\t" \
  "ds_read_b128 v[116:119], %2 offset:63360\n\t" \
  "v_exp_f32 v152, v152\n\t" \
  "v_exp_f32 v153, v153\n\t" \
  "v_exp_f32 v154, v154\n\t" \
  "v_smfmac_f32_32x32x32_bf16 %1, v[120:123], v[136:143], %10\n\t" \
  "ds_read_b128 v[120:123], %3 offset:61248\n\t" \
  "v_exp_f32 v155, v155\n\t" \
  "v_exp_f32 v156, v156\n\t" \
  "v_smfmac_f32_32x32x32_bf16 %1, v[128:131], v[144:151], %10\n\t" \
  "ds_read_b128 v[128:131], %3 offset:61792\n\t" \
  "v_exp_f32 v157, v157\n\t" \
  "v_exp_f32 v158, v158\n\t" \
  "v_exp_f32 v159, v159\n\t" \
  "v_exp_f32 v160, v160\n\t" \
  "v_exp_f32 v161, v161\n\t" \
  "v_exp_f32 v162, v162\n\t" \
  "v_exp_f32 v163, v163\n\t" \
  "v_exp_f32 v164, v164\n\t" \
  "v_exp_f32 v165, v165\n\t" \
  "v_exp_f32 v166, v166\n\t" \
  "v_exp_f32 v167, v167\n\t" \
  "v_mfma_f32_32x32x16_bf16 v[136:151], v[112:115], %6, %8\n\t" \
  "v_exp_f32 v96, v96\n\t" \
  "v_exp_f32 v97, v97\n\t" \
  "v_exp_f32 v98, v98\n\t" \
  "v_exp_f32 v99, v99\n\t" \
  "v_smfmac_f32_32x32x32_bf16 %0, v[124:127], v[152:159], %10\n\t" \
  "v_exp_f32 v100, v100\n\t" \
  "v_exp_f32 v101, v101\n\t" \
  "v_smfmac_f32_32x32x32_bf16 %0, v[132:135], v[160:167], %10\n\t" \
  "s_waitcnt lgkmcnt(0)\n\t" \
  "v_exp_f32 v102, v102\n\t" \
  "v_exp_f32 v103, v103\n\t" \
  "v_exp_f32 v104, v104\n\t" \
  "v_exp_f32 v105, v105\n\t" \
  "v_exp_f32 v106, v106\n\t" \
  "v_exp_f32 v107, v107\n\t" \
  "v_exp_f32 v108, v108\n\t" \
  "v_exp_f32 v109, v109\n\t" \
  "v_exp_f32 v110, v110\n\t" \
  "v_exp_f32 v111, v111\n\t" \
  "v_mfma_f32_32x32x16_bf16 v[152:167], v[112:115], %7, %9\n\t" \
  "ds_read_b128 v[112:115], %2 offset:65472\n\t" \
  "v_exp_f32 v136, v136\n\t" \
  "v_exp_f32 v137, v137\n\t" \
  "v_exp_f32 v138, v138\n\t" \
  "v_smfmac_f32_32x32x32_bf16 %1, v[124:127], v[96:103], %10\n\t" \
  "ds_read_b128 v[124:127], %3 offset:63360\n\t" \
  "v_exp_f32 v139, v139\n\t" \
  "v_exp_f32 v140, v140\n\t" \
  "v_smfmac_f32_32x32x32_bf16 %1, v[132:135], v[104:111], %10\n\t" \
  "ds_read_b128 v[132:135], %3 offset:63904\n\t" \
  "v_exp_f32 v141, v141\n\t" \
  "v_exp_f32 v142, v142\n\t" \
  "v_exp_f32 v143, v143\n\t" \
  "v_exp_f32 v144, v144\n\t" \
  "v_exp_f32 v145, v145\n\t" \
  "v_exp_f32 v146, v146\n\t" \
  "v_exp_f32 v147, v147\n\t" \
  "v_exp_f32 v148, v148\n\t" \
  "v_exp_f32 v149, v149\n\t" \
  "v_exp_f32 v150, v150\n\t" \
  "v_exp_f32 v151, v151\n\t" \
  "v_mfma_f32_32x32x16_bf16 v[96:111], v[116:119], %6, %8\n\t" \
  "v_exp_f32 v152, v152\n\t" \
  "v_exp_f32 v153, v153\n\t" \
  "v_exp_f32 v154, v154\n\t" \
  "v_exp_f32 v155, v155\n\t" \
  "v_smfmac_f32_32x32x32_bf16 %0, v[120:123], v[136:143], %10\n\t" \
  "v_exp_f32 v156, v156\n\t" \
  "v_exp_f32 v157, v157\n\t" \
  "v_smfmac_f32_32x32x32_bf16 %0, v[128:131], v[144:151], %10\n\t" \
  "s_waitcnt lgkmcnt(0)\n\t" \
  "v_exp_f32 v158, v158\n\t" \
  "v_exp_f32 v159, v159\n\t" \
  "v_exp_f32 v160, v160\n\t" \
  "v_exp_f32 v161, v161\n\t" \
  "v_exp_f32 v162, v162\n\t" \
  "v_exp_f32 v163, v163\n\t" \
  "v_exp_f32 v164, v164\n\t" \
  "v_exp_f32 v165, v165\n\t" \
  "v_exp_f32 v166, v166\n\t" \
  "v_exp_f32 v167, v167\n\t" \
  "v_mfma_f32_32x32x16_bf16 v[136:151], v[116:119], %7, %9\n\t" \
  "v_add_u32 %2, 0x10800, %2\n\t" \
  "ds_read_b128 v[116:119], %2 offset:0\n\t" \
  "v_exp_f32 v96, v96\n\t" \
  "v_exp_f32 v97, v97\n\t" \
  "v_exp_f32 v98, v98\n\t" \
  "v_smfmac_f32_32x32x32_bf16 %1, v[120:123], v[152:159], %10\n\t" \
  "v_add_u32 %3, 0xffc0, %3\n\t" \
  "ds_read_b128 v[120:123], %3 offset:0\n\t" \
  "v_exp_f32 v99, v99\n\t" \
  "v_exp_f32 v100, v100\n\t" \
  "v_smfmac_f32_32x32x32_bf16 %1, v[128:131], v[160:167], %10\n\t" \
  "ds_read_b128 v[128:131], %3 offset:544\n\t" \
  "v_exp_f32 v101, v101\n\t" \
  "v_exp_f32 v102, v102\n\t" \
  "v_exp_f32 v103, v103\n\t" \
  "v_exp_f32 v104, v104\n\t" \
  "v_exp_f32 v105, v105\n\t" \
  "v_exp_f32 v106, v106\n\t" \
  "v_exp_f32 v107, v107\n\t" \
  "v_exp_f32 v108, v108\n\t" \
  "v_exp_f32 v109, v109\n\t" \
  "v_exp_f32 v110, v110\n\t" \
  "v_exp_f32 v111, v111\n\t" \
  "v_mfma_f32_32x32x16_bf16 v[152:167], v[112:115], %6, %8\n\t" \
  "v_exp_f32 v136, v136\n\t" \
  "v_exp_f32 v137, v137\n\t" \
  "v_exp_f32 v138, v138\n\t" \
  "v_exp_f32 v139, v139\n\t" \
  "v_smfmac_f32_32x32x32_bf16 %0, v[124:127], v[96:103], %10\n\t" \
  "v_exp_f32 v140, v140\n\t" \
  "v_exp_f32 v141, v141\n\t" \
  "v_smfmac_f32_32x32x32_bf16 %0, v[132:135], v[104:111], %10\n\t" \
  "s_waitcnt lgkmcnt(0)\n\t" \
  "v_exp_f32 v142, v142\n\t" \
  "v_exp_f32 v143, v143\n\t" \
  "v_exp_f32 v144, v144\n\t" \
  "v_exp_f32 v145, v145\n\t" \
  "v_exp_f32 v146, v146\n\t" \
  "v_exp_f32 v147, v147\n\t" \
  "v_exp_f32 v148, v148\n\t" \
  "v_exp_f32 v149, v149\n\t" \
  "v_exp_f32 v150, v150\n\t" \
  "v_exp_f32 v151, v151\n\t" \
  "v_mfma_f32_32x32x16_bf16 v[96:111], v[112:115], %7, %9\n\t" \
  "ds_read_b128 v[112:115], %2 offset:2112\n\t" \
  "v_exp_f32 v152, v152\n\t" \
  "v_exp_f32 v153, v153\n\t" \
  "v_exp_f32 v154, v154\n\t" \
  "v_smfmac_f32_32x32x32_bf16 %1, v[124:127], v[136:143], %10\n\t" \
  "ds_read_b128 v[124:127], %3 offset:2112\n\t" \
  "v_exp_f32 v155, v155\n\t" \
  "v_exp_f32 v156, v156\n\t" \
  "v_smfmac_f32_32x32x32_bf16 %1, v[132:135], v[144:151], %10\n\t" \
  "ds_read_b128 v[132:135], %3 offset:2656\n\t" \
  "v_exp_f32 v157, v157\n\t" \
  "v_exp_f32 v158, v158\n\t" \
  "v_exp_f32 v159, v159\n\t" \
  "v_exp_f32 v160, v160\n\t" \
  "v_exp_f32 v161, v161\n\t" \
  "v_exp_f32 v162, v162\n\t" \
  "v_exp_f32 v163, v163\n\t" \
  "v_exp_f32 v164, v164\n\t" \
  "v_exp_f32 v165, v165\n\t" \
  "v_exp_f32 v166, v166\n\t" \
  "v_exp_f32 v167, v167\n\t" \
  "v_mfma_f32_32x32x16_bf16 v[136:151], v[116:119], %6, %8\n\t" \
  "v_exp_f32 v96, v96\n\t" \
  "v_exp_f32 v97, v97\n\t" \
  "v_exp_f32 v98, v98\n\t" \
  "v_exp_f32 v99, v99\n\t" \
  "v_smfmac_f32_32x32x32_bf16 %0, v[120:123], v[152:159], %10\n\t" \
  "v_exp_f32 v100, v100\n\t" \
  "v_exp_f32 v101, v101\n\t" \
  "v_smfmac_f32_32x32x32_bf16 %0, v[128:131], v[160:167], %10\n\t" \
  "s_waitcnt lgkmcnt(0)\n\t" \
  "v_exp_f32 v102, v102\n\t" \
  "v_exp_f32 v103, v103\n\t" \
  "v_exp_f32 v104, v104\n\t" \
  "v_exp_f32 v105, v105\n\t" \
  "v_exp_f32 v106, v106\n\t" \
  "v_exp_f32 v107, v107\n\t" \
  "v_exp_f32 v108, v108\n\t" \
  "v_exp_f32 v109, v109\n\t" \
  "v_exp_f32 v110, v110\n\t" \
  "v_exp_f32 v111, v111\n\t" \
  "v_mfma_f32_32x32x16_bf16 v[152:167], v[116:119], %7, %9\n\t" \
  "ds_read_b128 v[116:119], %2 offset:4224\n\t" \
  "v_exp_f32 v136, v136\n\t" \
  "v_exp_f32 v137, v137\n\t" \
  "v_exp_f32 v138, v138\n\t" \
  "v_smfmac_f32_32x32x32_bf16 %1, v[120:123], v[96:103], %10\n\t" \
  "ds_read_b128 v[120:123], %3 offset:4224\n\t" \
  "v_exp_f32 v139, v139\n\t" \
  "v_exp_f32 v140, v140\n\t" \
  "v_smfmac_f32_32x32x32_bf16 %1, v[128:131], v[104:111], %10\n\t" \
  "ds_read_b128 v[128:131], %3 offset:4768\n\t" \
  "v_exp_f32 v141, v141\n\t" \
  "v_exp_f32 v142, v142\n\t" \
  "v_exp_f32 v143, v143\n\t" \
  "v_exp_f32 v144, v144\n\t" \
  "v_exp_f32 v145, v145\n\t" \
  "v_exp_f32 v146, v146\n\t" \
  "v_exp_f32 v147, v147\n\t" \
  "v_exp_f32 v148, v148\n\t" \
  "v_exp_f32 v149, v149\n\t" \
  "v_exp_f32 v150, v150\n\t" \
  "v_exp_f32 v151, v151\n\t" \
  "v_mfma_f32_32x32x16_bf16 v[96:111], v[112:115], %6, %8\n\t" \
  "v_exp_f32 v152, v152\n\t" \
  "v_exp_f32 v153, v153\n\t" \
  "v_exp_f32 v154, v154\n\t" \
  "v_exp_f32 v155, v155\n\t" \
  "v_smfmac_f32_32x32x32_bf16 %0, v[124:127], v[136:143], %10\n\t" \
  "v_exp_f32 v156, v156\n\t" \
  "v_exp_f32 v157, v157\n\t" \
  "v_smfmac_f32_32x32x32_bf16 %0, v[132:135], v[144:151], %10\n\t" \
  "s_waitcnt lgkmcnt(0)\n\t" \
  "v_exp_f32 v158, v158\n\t" \
  "v_exp_f32 v159, v159\n\t" \
  "v_exp_f32 v160, v160\n\t" \
  "v_exp_f32 v161, v161\n\t" \
  "v_exp_f32 v162, v162\n\t" \
  "v_exp_f32 v163, v163\n\t" \
  "v_exp_f32 v164, v164\n\t" \
  "v_exp_f32 v165, v165\n\t" \
  "v_exp_f32 v166, v166\n\t" \
  "v_exp_f32 v167, v167\n\t" \
  "v_mfma_f32_32x32x16_bf16 v[136:151], v[112:115], %7, %9\n\t" \
  "ds_read_b128 v[112:115], %2 offset:6336\n\t" \
  "v_exp_f32 v96, v96\n\t" \
  "v_exp_f32 v97, v97\n\t" \
  "v_exp_f32 v98, v98\n\t" \
  "v_smfmac_f32_32x32x32_bf16 %1, v[124:127], v[152:159], %10\n\t" \
  "ds_read_b128 v[124:127], %3 offset:6336\n\t" \
  "v_exp_f32 v99, v99\n\t" \
  "v_exp_f32 v100, v100\n\t" \
  "v_smfmac_f32_32x32x32_bf16 %1, v[132:135], v[160:167], %10\n\t" \
  "ds_read_b128 v[132:135], %3 offset:6880\n\t" \
  "v_exp_f32 v101, v101\n\t" \
  "v_exp_f32 v102, v102\n\t" \
  "v_exp_f32 v103, v103\n\t" \
  "v_exp_f32 v104, v104\n\t" \
  "v_exp_f32 v105, v105\n\t" \
  "v_exp_f32 v106, v106\n\t" \
  "v_exp_f32 v107, v107\n\t" \
  "v_exp_f32 v108, v108\n\t" \
  "v_exp_f32 v109, v109\n\t" \
  "v_exp_f32 v110, v110\n\t" \
  "v_exp_f32 v111, v111\n\t" \
  "v_mfma_f32_32x32x16_bf16 v[152:167], v[116:119], %6, %8\n\t" \
  "v_exp_f32 v136, v136\n\t" \
  "v_exp_f32 v137, v137\n\t" \
  "v_exp_f32 v138, v138\n\t" \
  "v_exp_f32 v139, v139\n\t" \
  "v_smfmac_f32_32x32x32_bf16 %0, v[120:123], v[96:103], %10\n\t" \
  "v_exp_f32 v140, v140\n\t" \
  "v_exp_f32 v141, v141\n\t" \
  "v_smfmac_f32_32x32x32_bf16 %0, v[128:131], v[104:111], %10\n\t" \
  "s_waitcnt lgkmcnt(0)\n\t" \
  "v_exp_f32 v142, v142\n\t" \
  "v_exp_f32 v143, v143\n\t" \
  "v_exp_f32 v144, v144\n\t" \
  "v_exp_f32 v145, v145\n\t" \
  "v_exp_f32 v146, v146\n\t" \
  "v_exp_f32 v147, v147\n\t" \
  "v_exp_f32 v148, v148\n\t" \
  "v_exp_f32 v149, v149\n\t" \
  "v_exp_f32 v150, v150\n\t" \
  "v_exp_f32 v151, v151\n\t" \
  "v_mfma_f32_32x32x16_bf16 v[96:111], v[116:119], %7, %9\n\t" \
  "ds_read_b128 v[116:119], %2 offset:8448\n\t" \
  "v_exp_f32 v152, v152\n\t" \
  "v_exp_f32 v153, v153\n\t" \
  "v_exp_f32 v154, v154\n\t" \
  "v_smfmac_f32_32x32x32_bf16 %1, v[120:123], v[136:143], %10\n\t" \
  "ds_read_b128 v[120:123], %3 offset:8448\n\t" \
  "v_exp_f32 v155, v155\n\t" \
  "v_exp_f32 v156, v156\n\t" \
  "v_smfmac_f32_32x32x32_bf16 %1, v[128:131], v[144:151], %10\n\t" \
  "ds_read_b128 v[128:131], %3 offset:8992\n\t" \
  "v_exp_f32 v157, v157\n\t" \
  "v_exp_f32 v158, v158\n\t" \
  "v_exp_f32 v159, v159\n\t" \
  "v_exp_f32 v160, v160\n\t" \
  "v_exp_f32 v161, v161\n\t" \
  "v_exp_f32 v162, v162\n\t" \
  "v_exp_f32 v163, v163\n\t" \
  "v_exp_f32 v164, v164\n\t" \
  "v_exp_f32 v165, v165\n\t" \
  "v_exp_f32 v166, v166\n\t" \
  "v_exp_f32 v167, v167\n\t" \
  "v_mfma_f32_32x32x16_bf16 v[136:151], v[112:115], %6, %8\n\t" \
  "v_exp_f32 v96, v96\n\t" \
  "v_exp_f32 v97, v97\n\t" \
  "v_exp_f32 v98, v98\n\t" \
  "v_exp_f32 v99, v99\n\t" \
  "v_smfmac_f32_32x32x32_bf16 %0, v[124:127], v[152:159], %10\n\t" \
  "v_exp_f32 v100, v100\n\t" \
  "v_exp_f32 v101, v101\n\t" \
  "v_smfmac_f32_32x32x32_bf16 %0, v[132:135], v[160:167], %10\n\t" \
  "s_waitcnt lgkmcnt(0)\n\t" \
  "v_exp_f32 v102, v102\n\t" \
  "v_exp_f32 v103, v103\n\t" \
  "v_exp_f32 v104, v104\n\t" \
  "v_exp_f32 v105, v105\n\t" \
  "v_exp_f32 v106, v106\n\t" \
  "v_exp_f32 v107, v107\n\t" \
  "v_exp_f32 v108, v108\n\t" \
  "v_exp_f32 v109, v109\n\t" \
  "v_exp_f32 v110, v110\n\t" \
  "v_exp_f32 v111, v111\n\t" \
  "v_mfma_f32_32x32x16_bf16 v[152:167], v[112:115], %7, %9\n\t" \
  "ds_read_b128 v[112:115], %2 offset:10560\n\t" \
  "v_exp_f32 v136, v136\n\t" \
  "v_exp_f32 v137, v137\n\t" \
  "v_exp_f32 v138, v138\n\t" \
  "v_smfmac_f32_32x32x32_bf16 %1, v[124:127], v[96:103], %10\n\t" \
  "ds_read_b128 v[124:127], %3 offset:10560\n\t" \
  "v_exp_f32 v139, v139\n\t" \
  "v_exp_f32 v140, v140\n\t" \
  "v_smfmac_f32_32x32x32_bf16 %1, v[132:135], v[104:111], %10\n\t" \
  "ds_read_b128 v[132:135], %3 offset:11104\n\t" \
  "v_exp_f32 v141, v141\n\t" \
  "v_exp_f32 v142, v142\n\t" \
  "v_exp_f32 v143, v143\n\t" \
  "v_exp_f32 v144, v144\n\t" \
  "v_exp_f32 v145, v145\n\t" \
  "v_exp_f32 v146, v146\n\t" \
  "v_exp_f32 v147, v147\n\t" \
  "v_exp_f32 v148, v148\n\t" \
  "v_exp_f32 v149, v149\n\t" \
  "v_exp_f32 v150, v150\n\t" \
  "v_exp_f32 v151, v151\n\t" \
  "v_mfma_f32_32x32x16_bf16 v[96:111], v[116:119], %6, %8\n\t" \
  "ds_read_b32 %4, %11 offset:16\n\t" \
  "v_exp_f32 v152, v152\n\t" \
  "v_exp_f32 v153, v153\n\t" \
  "v_exp_f32 v154, v154\n\t" \
  "v_exp_f32 v155, v155\n\t" \
  "v_smfmac_f32_32x32x32_bf16 %0, v[120:123], v[136:143], %10\n\t" \
  "v_exp_f32 v156, v156\n\t" \
  "v_exp_f32 v157, v157\n\t" \
  "v_smfmac_f32_32x32x32_bf16 %0, v[128:131], v[144:151], %10\n\t" \
  "s_waitcnt lgkmcnt(0)\n\t" \
  "v_exp_f32 v158, v158\n\t" \
  "v_exp_f32 v159, v159\n\t" \
  "v_exp_f32 v160, v160\n\t" \
  "v_exp_f32 v161, v161\n\t" \
  "v_exp_f32 v162, v162\n\t" \
  "v_exp_f32 v163, v163\n\t" \
  "v_exp_f32 v164, v164\n\t" \
  "v_exp_f32 v165, v165\n\t" \
  "v_exp_f32 v166, v166\n\t" \
  "v_exp_f32 v167, v167\n\t" \
  "v_mfma_f32_32x32x16_bf16 v[136:151], v[116:119], %7, %9\n\t" \
  "v_readfirstlane_b32 %5, %4\n\t" \
  "s_nop 0\n\t" \
  "s_cmp_lt_u32 %5, 8\n\t" \
  "s_cbranch_scc1 14f\n\t" \
  "24:\n\t" \
  "ds_read_b128 v[116:119], %2 offset:12672\n\t" \
  "v_exp_f32 v96, v96\n\t" \
  "v_exp_f32 v97, v97\n\t" \
  "v_exp_f32 v98, v98\n\t" \
  "v_smfmac_f32_32x32x32_bf16 %1, v[120:123], v[152:159], %10\n\t" \
  "ds_read_b128 v[120:123], %3 offset:12672\n\t" \
  "v_exp_f32 v99, v99\n\t" \
  "v_exp_f32 v100, v100\n\t" \
  "v_smfmac_f32_32x32x32_bf16 %1, v[128:131], v[160:167], %10\n\t" \
  "ds_read_b128 v[128:131], %3 offset:13216\n\t" \
  "v_exp_f32 v101, v101\n\t" \
  "v_exp_f32 v102, v102\n\t" \
  "v_exp_f32 v103, v103\n\t" \
  "v_exp_f32 v104, v104\n\t" \
  "v_exp_f32 v105, v105\n\t" \
  "v_exp_f32 v106, v106\n\t" \
  "v_exp_f32 v107, v107\n\t" \
  "v_exp_f32 v108, v108\n\t" \
  "v_exp_f32 v109, v109\n\t" \
  "v_exp_f32 v110, v110\n\t" \
  "v_exp_f32 v111, v111\n\t" \
  "v_mfma_f32_32x32x16_bf16 v[152:167], v[112:115], %6, %8\n\t" \
  "v_exp_f32 v136, v136\n\t" \
  "v_exp_f32 v137, v137\n\t" \
  "v_exp_f32 v138, v138\n\t" \
  "v_exp_f32 v139, v139\n\t" \
  "v_smfmac_f32_32x32x32_bf16 %0, v[124:127], v[96:103], %10\n\t" \
  "v_exp_f32 v140, v140\n\t" \
  "v_exp_f32 v141, v141\n\t" \
  "v_smfmac_f32_32x32x32_bf16 %0, v[132:135], v[104:111], %10\n\t" \
  "s_waitcnt lgkmcnt(0)\n\t" \
  "v_exp_f32 v142, v142\n\t" \
  "v_exp_f32 v143, v143\n\t" \
  "v_exp_f32 v144, v144\n\t" \
  "v_exp_f32 v145, v145\n\t" \
  "v_exp_f32 v146, v146\n\t" \
  "v_exp_f32 v147, v147\n\t" \
  "v_exp_f32 v148, v148\n\t" \
  "v_exp_f32 v149, v149\n\t" \
  "v_exp_f32 v150, v150\n\t" \
  "v_exp_f32 v151, v151\n\t" \
  "v_mfma_f32_32x32x16_bf16 v[96:111], v[112:115], %7, %9\n\t" \
  "ds_read_b128 v[112:115], %2 offset:14784\n\t" \
  "v_exp_f32 v152, v152\n\t" \
  "v_exp_f32 v153, v153\n\t" \
  "v_exp_f32 v154, v154\n\t" \
  "v_smfmac_f32_32x32x32_bf16 %1, v[124:127], v[136:143], %10\n\t" \
  "ds_read_b128 v[124:127], %3 offset:14784\n\t" \
  "v_exp_f32 v155, v155\n\t" \
  "v_exp_f32 v156, v156\n\t" \
  "v_smfmac_f32_32x32x32_bf16 %1, v[132:135], v[144:151], %10\n\t" \
  "ds_read_b128 v[132:135], %3 offset:15328\n\t" \
  "v_exp_f32 v157, v157\n\t" \
  "v_exp_f32 v158, v158\n\t" \
  "v_exp_f32 v159, v159\n\t" \
  "v_exp_f32 v160, v160\n\t" \
  "v_exp_f32 v161, v161\n\t" \
  "v_exp_f32 v162, v162\n\t" \
  "v_exp_f32 v163, v163\n\t" \
  "v_exp_f32 v164, v164\n\t" \
  "v_exp_f32 v165, v165\n\t" \
  "v_exp_f32 v166, v166\n\t" \
  "v_exp_f32 v167, v167\n\t" \
  "v_mfma_f32_32x32x16_bf16 v[136:151], v[116:119], %6, %8\n\t" \
  "v_exp_f32 v96, v96\n\t" \
  "v_exp_f32 v97, v97\n\t" \
  "v_exp_f32 v98, v98\n\t" \
  "v_exp_f32 v99, v99\n\t" \
  "v_smfmac_f32_32x32x32_bf16 %0, v[120:123], v[152:159], %10\n\t" \
  "v_exp_f32 v100, v100\n\t" \
  "v_exp_f32 v101, v101\n\t" \
  "v_smfmac_f32_32x32x32_bf16 %0, v[128:131], v[160:167], %10\n\t" \
  "s_waitcnt lgkmcnt(0)\n\t" \
  "v_exp_f32 v102, v102\n\t" \
  "v_exp_f32 v103, v103\n\t" \
  "v_exp_f32 v104, v104\n\t" \
  "v_exp_f32 v105, v105\n\t" \
  "v_exp_f32 v106, v106\n\t" \
  "v_exp_f32 v107, v107\n\t" \
  "v_exp_f32 v108, v108\n\t" \
  "v_exp_f32 v109, v109\n\t" \
  "v_exp_f32 v110, v110\n\t" \
  "v_exp_f32 v111, v111\n\t" \
  "v_mfma_f32_32x32x16_bf16 v[152:167], v[116:119], %7, %9\n\t" \
  "ds_read_b128 v[116:119], %2 offset:16896\n\t" \
  "v_exp_f32 v136, v136\n\t" \
  "v_exp_f32 v137, v137\n\t" \
  "v_exp_f32 v138, v138\n\t" \
  "v_smfmac_f32_32x32x32_bf16 %1, v[120:123], v[96:103], %10\n\t" \
  "ds_read_b128 v[120:123], %3 offset:16896\n\t" \
  "v_exp_f32 v139, v139\n\t" \
  "v_exp_f32 v140, v140\n\t" \
  "v_smfmac_f32_32x32x32_bf16 %1, v[128:131], v[104:111], %10\n\t" \
  "ds_read_b128 v[128:131], %3 offset:17440\n\t" \
  "v_exp_f32 v141, v141\n\t" \
  "v_exp_f32 v142, v142\n\t" \
  "v_exp_f32 v143, v143\n\t" \
  "v_exp_f32 v144, v144\n\t" \
  "v_exp_f32 v145, v145\n\t" \
  "v_exp_f32 v146, v146\n\t" \
  "v_exp_f32 v147, v147\n\t" \
  "v_exp_f32 v148, v148\n\t" \
  "v_exp_f32 v149, v149\n\t" \
  "v_exp_f32 v150, v150\n\t" \
  "v_exp_f32 v151, v151\n\t" \
  "v_mfma_f32_32x32x16_bf16 v[96:111], v[112:115], %6, %8\n\t" \
  "v_mov_b32 %4, 1\n\t" \
  "s_mov_b64 exec, 1\n\t" \
  "ds_add_u32 %11, %4 offset:48\n\t" \
  "s_mov_b64 exec, -1\n\t" \
  "v_exp_f32 v152, v152\n\t" \
  "v_exp_f32 v153, v153\n\t" \
  "v_exp_f32 v154, v154\n\t" \
  "v_exp_f32 v155, v155\n\t" \
  "v_smfmac_f32_32x32x32_bf16 %0, v[124:127], v[136:143], %10\n\t" \
  "v_exp_f32 v156, v156\n\t" \
  "v_exp_f32 v157, v157\n\t" \
  "v_smfmac_f32_32x32x32_bf16 %0, v[132:135], v[144:151], %10\n\t" \
  "s_waitcnt lgkmcnt(0)\n\t" \
  "v_exp_f32 v158, v158\n\t" \
  "v_exp_f32 v159, v159\n\t" \
  "v_exp_f32 v160, v160\n\t" \
  "v_exp_f32 v161, v161\n\t" \
  "v_exp_f32 v162, v162\n\t" \
  "v_exp_f32 v163, v163\n\t" \
  "v_exp_f32 v164, v164\n\t" \
  "v_exp_f32 v165, v165\n\t" \
  "v_exp_f32 v166, v166\n\t" \
  "v_exp_f32 v167, v167\n\t" \
  "v_mfma_f32_32x32x16_bf16 v[136:151], v[112:115], %7, %9\n\t" \
  "ds_read_b128 v[112:115], %2 offset:19008\n\t" \
  "v_exp_f32 v96, v96\n\t" \
  "v_exp_f32 v97, v97\n\t" \
  "v_exp_f32 v98, v98\n\t" \
  "v_smfmac_f32_32x32x32_bf16 %1, v[124:127], v[152:159], %10\n\t" \
  "ds_read_b128 v[124:127], %3 offset:19008\n\t" \
  "v_exp_f32 v99, v99\n\t" \
  "v_exp_f32 v100, v100\n\t" \
  "v_smfmac_f32_32x32x32_bf16 %1, v[132:135], v[160:167], %10\n\t" \
  "ds_read_b128 v[132:135], %3 offset:19552\n\t" \
  "v_exp_f32 v101, v101\n\t" \
  "v_exp_f32 v102, v102\n\t" \
  "v_exp_f32 v103, v103\n\t" \
  "v_exp_f32 v104, v104\n\t" \
  "v_exp_f32 v105, v105\n\t" \
  "v_exp_f32 v106, v106\n\t" \
  "v_exp_f32 v107, v107\n\t" \
  "v_exp_f32 v108, v108\n\t" \
  "v_exp_f32 v109, v109\n\t" \
  "v_exp_f32 v110, v110\n\t" \
  "v_exp_f32 v111, v111\n\t" \
  "v_mfma_f32_32x32x16_bf16 v[152:167], v[116:119], %6, %8\n\t" \
  "v_exp_f32 v136, v136\n\t" \
  "v_exp_f32 v137, v137\n\t" \
  "v_exp_f32 v138, v138\n\t" \
  "v_exp_f32 v139, v139\n\t" \
  "v_smfmac_f32_32x32x32_bf16 %0, v[120:123], v[96:103], %10\n\t" \
  "v_exp_f32 v140, v140\n\t" \
  "v_exp_f32 v141, v141\n\t" \
  "v_smfmac_f32_32x32x32_bf16 %0, v[128:131], v[104:111], %10\n\t" \
  "s_waitcnt lgkmcnt(0)\n\t" \
  "v_exp_f32 v142, v142\n\t" \
  "v_exp_f32 v143, v143\n\t" \
  "v_exp_f32 v144, v144\n\t" \
  "v_exp_f32 v145, v145\n\t" \
  "v_exp_f32 v146, v146\n\t" \
  "v_exp_f32 v147, v147\n\t" \
  "v_exp_f32 v148, v148\n\t" \
  "v_exp_f32 v149, v149\n\t" \
  "v_exp_f32 v150, v150\n\t" \
  "v_exp_f32 v151, v151\n\t" \
  "v_mfma_f32_32x32x16_bf16 v[96:111], v[116:119], %7, %9\n\t" \
  "ds_read_b128 v[116:119], %2 offset:21120\n\t" \
  "v_exp_f32 v152, v152\n\t" \
  "v_exp_f32 v153, v153\n\t" \
  "v_exp_f32 v154, v154\n\t" \
  "v_smfmac_f32_32x32x32_bf16 %1, v[120:123], v[136:143], %10\n\t" \
  "ds_read_b128 v[120:123], %3 offset:21120\n\t" \
  "v_exp_f32 v155, v155\n\t" \
  "v_exp_f32 v156, v156\n\t" \
  "v_smfmac_f32_32x32x32_bf16 %1, v[128:131], v[144:151], %10\n\t" \
  "ds_read_b128 v[128:131], %3 offset:21664\n\t" \
  "v_exp_f32 v157, v157\n\t" \
  "v_exp_f32 v158, v158\n\t" \
  "v_exp_f32 v159, v159\n\t" \
  "v_exp_f32 v160, v160\n\t" \
  "v_exp_f32 v161, v161\n\t" \
  "v_exp_f32 v162, v162\n\t" \
  "v_exp_f32 v163, v163\n\t" \
  "v_exp_f32 v164, v164\n\t" \
  "v_exp_f32 v165, v165\n\t" \
  "v_exp_f32 v166, v166\n\t" \
  "v_exp_f32 v167, v167\n\t" \
  "v_mfma_f32_32x32x16_bf16 v[136:151], v[112:115], %6, %8\n\t" \
  "v_exp_f32 v96, v96\n\t" \
  "v_exp_f32 v97, v97\n\t" \
  "v_exp_f32 v98, v98\n\t" \
  "v_exp_f32 v99, v99\n\t" \
  "v_smfmac_f32_32x32x32_bf16 %0, v[124:127], v[152:159], %10\n\t" \
  "v_exp_f32 v100, v100\n\t" \
  "v_exp_f32 v101, v101\n\t" \
  "v_smfmac_f32_32x32x32_bf16 %0, v[132:135], v[160:167], %10\n\t" \
  "s_waitcnt lgkmcnt(0)\n\t" \
  "v_exp_f32 v102, v102\n\t" \
  "v_exp_f32 v103, v103\n\t" \
  "v_exp_f32 v104, v104\n\t" \
  "v_exp_f32 v105, v105\n\t" \
  "v_exp_f32 v106, v106\n\t" \
  "v_exp_f32 v107, v107\n\t" \
  "v_exp_f32 v108, v108\n\t" \
  "v_exp_f32 v109, v109\n\t" \
  "v_exp_f32 v110, v110\n\t" \
  "v_exp_f32 v111, v111\n\t" \
  "v_mfma_f32_32x32x16_bf16 v[152:167], v[112:115], %7, %9\n\t" \
  "ds_read_b128 v[112:115], %2 offset:23232\n\t" \
  "v_exp_f32 v136, v136\n\t" \
  "v_exp_f32 v137, v137\n\t" \
  "v_exp_f32 v138, v138\n\t" \
  "v_smfmac_f32_32x32x32_bf16 %1, v[124:127], v[96:103], %10\n\t" \
  "ds_read_b128 v[124:127], %3 offset:23232\n\t" \
  "v_exp_f32 v139, v139\n\t" \
  "v_exp_f32 v140, v140\n\t" \
  "v_smfmac_f32_32x32x32_bf16 %1, v[132:135], v[104:111], %10\n\t" \
  "ds_read_b128 v[132:135], %3 offset:23776\n\t" \
  "v_exp_f32 v141, v141\n\t" \
  "v_exp_f32 v142, v142\n\t" \
  "v_exp_f32 v143, v143\n\t" \
  "v_exp_f32 v144, v144\n\t" \
  "v_exp_f32 v145, v145\n\t" \
  "v_exp_f32 v146, v146\n\t" \
  "v_exp_f32 v147, v147\n\t" \
  "v_exp_f32 v148, v148\n\t" \
  "v_exp_f32 v149, v149\n\t" \
  "v_exp_f32 v150, v150\n\t" \
  "v_exp_f32 v151, v151\n\t" \
  "v_mfma_f32_32x32x16_bf16 v[96:111], v[116:119], %6, %8\n\t" \
  "v_exp_f32 v152, v152\n\t" \
  "v_exp_f32 v153, v153\n\t" \
  "v_exp_f32 v154, v154\n\t" \
  "v_exp_f32 v155, v155\n\t" \
  "v_smfmac_f32_32x32x32_bf16 %0, v[120:123], v[136:143], %10\n\t" \
  "v_exp_f32 v156, v156\n\t" \
  "v_exp_f32 v157, v157\n\t" \
  "v_smfmac_f32_32x32x32_bf16 %0, v[128:131], v[144:151], %10\n\t" \
  "s_waitcnt lgkmcnt(0)\n\t" \
  "v_exp_f32 v158, v158\n\t" \
  "v_exp_f32 v159, v159\n\t" \
  "v_exp_f32 v160, v160\n\t" \
  "v_exp_f32 v161, v161\n\t" \
  "v_exp_f32 v162, v162\n\t" \
  "v_exp_f32 v163, v163\n\t" \
  "v_exp_f32 v164, v164\n\t" \
  "v_exp_f32 v165, v165\n\t" \
  "v_exp_f32 v166, v166\n\t" \
  "v_exp_f32 v167, v167\n\t" \
  "v_mfma_f32_32x32x16_bf16 v[136:151], v[116:119], %7, %9\n\t" \
  "ds_read_b128 v[116:119], %2 offset:25344\n\t" \
  "v_exp_f32 v96, v96\n\t" \
  "v_exp_f32 v97, v97\n\t" \
  "v_exp_f32 v98, v98\n\t" \
  "v_smfmac_f32_32x32x32_bf16 %1, v[120:123], v[152:159], %10\n\t" \
  "ds_read_b128 v[120:123], %3 offset:25344\n\t" \
  "v_exp_f32 v99, v99\n\t" \
  "v_exp_f32 v100, v100\n\t" \
  "v_smfmac_f32_32x32x32_bf16 %1, v[128:131], v[160:167], %10\n\t" \
  "ds_read_b128 v[128:131], %3 offset:25888\n\t" \
  "v_exp_f32 v101, v101\n\t" \
  "v_exp_f32 v102, v102\n\t" \
  "v_exp_f32 v103, v103\n\t" \
  "v_exp_f32 v104, v104\n\t" \
  "v_exp_f32 v105, v105\n\t" \
  "v_exp_f32 v106, v106\n\t" \
  "v_exp_f32 v107, v107\n\t" \
  "v_exp_f32 v108, v108\n\t" \
  "v_exp_f32 v109, v109\n\t" \
  "v_exp_f32 v110, v110\n\t" \
  "v_exp_f32 v111, v111\n\t" \
  "v_mfma_f32_32x32x16_bf16 v[152:167], v[112:115], %6, %8\n\t" \
  "v_exp_f32 v136, v136\n\t" \
  "v_exp_f32 v137, v137\n\t" \
  "v_exp_f32 v138, v138\n\t" \
  "v_exp_f32 v139, v139\n\t" \
  "v_smfmac_f32_32x32x32_bf16 %0, v[124:127], v[96:103], %10\n\t" \
  "v_exp_f32 v140, v140\n\t" \
  "v_exp_f32 v141, v141\n\t" \
  "v_smfmac_f32_32x32x32_bf16 %0, v[132:135], v[104:111], %10\n\t" \
  "s_waitcnt lgkmcnt(0)\n\t" \
  "v_exp_f32 v142, v142\n\t" \
  "v_exp_f32 v143, v143\n\t" \
  "v_exp_f32 v144, v144\n\t" \
  "v_exp_f32 v145, v145\n\t" \
  "v_exp_f32 v146, v146\n\t" \
  "v_exp_f32 v147, v147\n\t" \
  "v_exp_f32 v148, v148\n\t" \
  "v_exp_f32 v149, v149\n\t" \
  "v_exp_f32 v150, v150\n\t" \
  "v_exp_f32 v151, v151\n\t" \
  "v_mfma_f32_32x32x16_bf16 v[96:111], v[112:115], %7, %9\n\t" \
  "ds_read_b128 v[112:115], %2 offset:27456\n\t" \
  "v_exp_f32 v152, v152\n\t" \
  "v_exp_f32 v153, v153\n\t" \
  "v_exp_f32 v154, v154\n\t" \
  "v_smfmac_f32_32x32x32_bf16 %1, v[124:127], v[136:143], %10\n\t" \
  "ds_read_b128 v[124:127], %3 offset:27456\n\t" \
  "v_exp_f32 v155, v155\n\t" \
  "v_exp_f32 v156, v156\n\t" \
  "v_smfmac_f32_32x32x32_bf16 %1, v[132:135], v[144:151], %10\n\t" \
  "ds_read_b128 v[132:135], %3 offset:28000\n\t" \
  "v_exp_f32 v157, v157\n\t" \
  "v_exp_f32 v158, v158\n\t" \
  "v_exp_f32 v159, v159\n\t" \
  "v_exp_f32 v160, v160\n\t" \
  "v_exp_f32 v161, v161\n\t" \
  "v_exp_f32 v162, v162\n\t" \
  "v_exp_f32 v163, v163\n\t" \
  "v_exp_f32 v164, v164\n\t" \
  "v_exp_f32 v165, v165\n\t" \
  "v_exp_f32 v166, v166\n\t" \
  "v_exp_f32 v167, v167\n\t" \
  "v_mfma_f32_32x32x16_bf16 v[136:151], v[116:119], %6, %8\n\t" \
  "v_exp_f32 v96, v96\n\t" \
  "v_exp_f32 v97, v97\n\t" \
  "v_exp_f32 v98, v98\n\t" \
  "v_exp_f32 v99, v99\n\t" \
  "v_smfmac_f32_32x32x32_bf16 %0, v[120:123], v[152:159], %10\n\t" \
  "v_exp_f32 v100, v100\n\t" \
  "v_exp_f32 v101, v101\n\t" \
  "v_smfmac_f32_32x32x32_bf16 %0, v[128:131], v[160:167], %10\n\t" \
  "s_waitcnt lgkmcnt(0)\n\t" \
  "v_exp_f32 v102, v102\n\t" \
  "v_exp_f32 v103, v103\n\t" \
  "v_exp_f32 v104, v104\n\t" \
  "v_exp_f32 v105, v105\n\t" \
  "v_exp_f32 v106, v106\n\t" \
  "v_exp_f32 v107, v107\n\t" \
  "v_exp_f32 v108, v108\n\t" \
  "v_exp_f32 v109, v109\n\t" \
  "v_exp_f32 v110, v110\n\t" \
  "v_exp_f32 v111, v111\n\t" \
  "v_mfma_f32_32x32x16_bf16 v[152:167], v[116:119], %7, %9\n\t" \
  "ds_read_b128 v[116:119], %2 offset:29568\n\t" \
  "v_exp_f32 v136, v136\n\t" \
  "v_exp_f32 v137, v137\n\t" \
  "v_exp_f32 v138, v138\n\t" \
  "v_smfmac_f32_32x32x32_bf16 %1, v[120:123], v[96:103], %10\n\t" \
  "ds_read_b128 v[120:123], %3 offset:29568\n\t" \
  "v_exp_f32 v139, v139\n\t" \
  "v_exp_f32 v140, v140\n\t" \
  "v_smfmac_f32_32x32x32_bf16 %1, v[128:131], v[104:111], %10\n\t" \
  "ds_read_b128 v[128:131], %3 offset:30112\n\t" \
  "v_exp_f32 v141, v141\n\t" \
  "v_exp_f32 v142, v142\n\t" \
  "v_exp_f32 v143, v143\n\t" \
  "v_exp_f32 v144, v144\n\t" \
  "v_exp_f32 v145, v145\n\t" \
  "v_exp_f32 v146, v146\n\t" \
  "v_exp_f32 v147, v147\n\t" \
  "v_exp_f32 v148, v148\n\t" \
  "v_exp_f32 v149, v149\n\t" \
  "v_exp_f32 v150, v150\n\t" \
  "v_exp_f32 v151, v151\n\t" \
  "v_mfma_f32_32x32x16_bf16 v[96:111], v[112:115], %6, %8\n\t" \
  "v_exp_f32 v152, v152\n\t" \
  "v_exp_f32 v153, v153\n\t" \
  "v_exp_f32 v154, v154\n\t" \
  "v_exp_f32 v155, v155\n\t" \
  "v_smfmac_f32_32x32x32_bf16 %0, v[124:127], v[136:143], %10\n\t" \
  "v_exp_f32 v156, v156\n\t" \
  "v_exp_f32 v157, v157\n\t" \
  "v_smfmac_f32_32x32x32_bf16 %0, v[132:135], v[144:151], %10\n\t" \
  "s_waitcnt lgkmcnt(0)\n\t" \
  "v_exp_f32 v158, v158\n\t" \
  "v_exp_f32 v159, v159\n\t" \
  "v_exp_f32 v160, v160\n\t" \
  "v_exp_f32 v161, v161\n\t" \
  "v_exp_f32 v162, v162\n\t" \
  "v_exp_f32 v163, v163\n\t" \
  "v_exp_f32 v164, v164\n\t" \
  "v_exp_f32 v165, v165\n\t" \
  "v_exp_f32 v166, v166\n\t" \
  "v_exp_f32 v167, v167\n\t" \
  "v_mfma_f32_32x32x16_bf16 v[136:151], v[112:115], %7, %9\n\t" \
  "ds_read_b128 v[112:115], %2 offset:31680\n\t" \
  "v_exp_f32 v96, v96\n\t" \
  "v_exp_f32 v97, v97\n\t" \
  "v_exp_f32 v98, v98\n\t" \
  "v_smfmac_f32_32x32x32_bf16 %1, v[124:127], v[152:159], %10\n\t" \
  "ds_read_b128 v[124:127], %3 offset:31680\n\t" \
  "v_exp_f32 v99, v99\n\t" \
  "v_exp_f32 v100, v100\n\t" \
  "v_smfmac_f32_32x32x32_bf16 %1, v[132:135], v[160:167], %10\n\t" \
  "ds_read_b128 v[132:135], %3 offset:32224\n\t" \
  "v_exp_f32 v101, v101\n\t" \
  "v_exp_f32 v102, v102\n\t" \
  "v_exp_f32 v103, v103\n\t" \
  "v_exp_f32 v104, v104\n\t" \
  "v_exp_f32 v105, v105\n\t" \
  "v_exp_f32 v106, v106\n\t" \
  "v_exp_f32 v107, v107\n\t" \
  "v_exp_f32 v108, v108\n\t" \
  "v_exp_f32 v109, v109\n\t" \
  "v_exp_f32 v110, v110\n\t" \
  "v_exp_f32 v111, v111\n\t" \
  "v_mfma_f32_32x32x16_bf16 v[152:167], v[116:119], %6, %8\n\t" \
  "v_exp_f32 v136, v136\n\t" \
  "v_exp_f32 v137, v137\n\t" \
  "v_exp_f32 v138, v138\n\t" \
  "v_exp_f32 v139, v139\n\t" \
  "v_smfmac_f32_32x32x32_bf16 %0, v[120:123], v[96:103], %10\n\t" \
  "v_exp_f32 v140, v140\n\t" \
  "v_exp_f32 v141, v141\n\t" \
  "v_smfmac_f32_32x32x32_bf16 %0, v[128:131], v[104:111], %10\n\t" \
  "s_waitcnt lgkmcnt(0)\n\t" \
  "v_exp_f32 v142, v142\n\t" \
  "v_exp_f32 v143, v143\n\t" \
  "v_exp_f32 v144, v144\n\t" \
  "v_exp_f32 v145, v145\n\t" \
  "v_exp_f32 v146, v146\n\t" \
  "v_exp_f32 v147, v147\n\t" \
  "v_exp_f32 v148, v148\n\t" \
  "v_exp_f32 v149, v149\n\t" \
  "v_exp_f32 v150, v150\n\t" \
  "v_exp_f32 v151, v151\n\t" \
  "v_mfma_f32_32x32x16_bf16 v[96:111], v[116:119], %7, %9\n\t" \
  "ds_read_b128 v[116:119], %2 offset:33792\n\t" \
  "v_exp_f32 v152, v152\n\t" \
  "v_exp_f32 v153, v153\n\t" \
  "v_exp_f32 v154, v154\n\t" \
  "v_smfmac_f32_32x32x32_bf16 %1, v[120:123], v[136:143], %10\n\t" \
  "ds_read_b128 v[120:123], %3 offset:33792\n\t" \
  "v_exp_f32 v155, v155\n\t" \
  "v_exp_f32 v156, v156\n\t" \
  "v_smfmac_f32_32x32x32_bf16 %1, v[128:131], v[144:151], %10\n\t" \
  "ds_read_b128 v[128:131], %3 offset:34336\n\t" \
  "v_exp_f32 v157, v157\n\t" \
  "v_exp_f32 v158, v158\n\t" \
  "v_exp_f32 v159, v159\n\t" \
  "v_exp_f32 v160, v160\n\t" \
  "v_exp_f32 v161, v161\n\t" \
  "v_exp_f32 v162, v162\n\t" \
  "v_exp_f32 v163, v163\n\t" \
  "v_exp_f32 v164, v164\n\t" \
  "v_exp_f32 v165, v165\n\t" \
  "v_exp_f32 v166, v166\n\t" \
  "v_exp_f32 v167, v167\n\t" \
  "v_mfma_f32_32x32x16_bf16 v[136:151], v[112:115], %6, %8\n\t" \
  "v_exp_f32 v96, v96\n\t" \
  "v_exp_f32 v97, v97\n\t" \
  "v_exp_f32 v98, v98\n\t" \
  "v_exp_f32 v99, v99\n\t" \
  "v_smfmac_f32_32x32x32_bf16 %0, v[124:127], v[152:159], %10\n\t" \
  "v_exp_f32 v100, v100\n\t" \
  "v_exp_f32 v101, v101\n\t" \
  "v_smfmac_f32_32x32x32_bf16 %0, v[132:135], v[160:167], %10\n\t" \
  "s_waitcnt lgkmcnt(0)\n\t" \
  "v_exp_f32 v102, v102\n\t" \
  "v_exp_f32 v103, v103\n\t" \
  "v_exp_f32 v104, v104\n\t" \
  "v_exp_f32 v105, v105\n\t" \
  "v_exp_f32 v106, v106\n\t" \
  "v_exp_f32 v107, v107\n\t" \
  "v_exp_f32 v108, v108\n\t" \
  "v_exp_f32 v109, v109\n\t" \
  "v_exp_f32 v110, v110\n\t" \
  "v_exp_f32 v111, v111\n\t" \
  "v_mfma_f32_32x32x16_bf16 v[152:167], v[112:115], %7, %9\n\t" \
  "ds_read_b128 v[112:115], %2 offset:35904\n\t" \
  "v_exp_f32 v136, v136\n\t" \
  "v_exp_f32 v137, v137\n\t" \
  "v_exp_f32 v138, v138\n\t" \
  "v_smfmac_f32_32x32x32_bf16 %1, v[124:127], v[96:103], %10\n\t" \
  "ds_read_b128 v[124:127], %3 offset:35904\n\t" \
  "v_exp_f32 v139, v139\n\t" \
  "v_exp_f32 v140, v140\n\t" \
  "v_smfmac_f32_32x32x32_bf16 %1, v[132:135], v[104:111], %10\n\t" \
  "ds_read_b128 v[132:135], %3 offset:36448\n\t" \
  "v_exp_f32 v141, v141\n\t" \
  "v_exp_f32 v142, v142\n\t" \
  "v_exp_f32 v143, v143\n\t" \
  "v_exp_f32 v144, v144\n\t" \
  "v_exp_f32 v145, v145\n\t" \
  "v_exp_f32 v146, v146\n\t" \
  "v_exp_f32 v147, v147\n\t" \
  "v_exp_f32 v148, v148\n\t" \
  "v_exp_f32 v149, v149\n\t" \
  "v_exp_f32 v150, v150\n\t" \
  "v_exp_f32 v151, v151\n\t" \
  "v_mfma_f32_32x32x16_bf16 v[96:111], v[116:119], %6, %8\n\t" \
  "v_exp_f32 v152, v152\n\t" \
  "v_exp_f32 v153, v153\n\t" \
  "v_exp_f32 v154, v154\n\t" \
  "v_exp_f32 v155, v155\n\t" \
  "v_smfmac_f32_32x32x32_bf16 %0, v[120:123], v[136:143], %10\n\t" \
  "v_exp_f32 v156, v156\n\t" \
  "v_exp_f32 v157, v157\n\t" \
  "v_smfmac_f32_32x32x32_bf16 %0, v[128:131], v[144:151], %10\n\t" \
  "s_waitcnt lgkmcnt(0)\n\t" \
  "v_exp_f32 v158, v158\n\t" \
  "v_exp_f32 v159, v159\n\t" \
  "v_exp_f32 v160, v160\n\t" \
  "v_exp_f32 v161, v161\n\t" \
  "v_exp_f32 v162, v162\n\t" \
  "v_exp_f32 v163, v163\n\t" \
  "v_exp_f32 v164, v164\n\t" \
  "v_exp_f32 v165, v165\n\t" \
  "v_exp_f32 v166, v166\n\t" \
  "v_exp_f32 v167, v167\n\t" \
  "v_mfma_f32_32x32x16_bf16 v[136:151], v[116:119], %7, %9\n\t" \
  "ds_read_b128 v[116:119], %2 offset:38016\n\t" \
  "v_exp_f32 v96, v96\n\t" \
  "v_exp_f32 v97, v97\n\t" \
  "v_exp_f32 v98, v98\n\t" \
  "v_smfmac_f32_32x32x32_bf16 %1, v[120:123], v[152:159], %10\n\t" \
  "ds_read_b128 v[120:123], %3 offset:38016\n\t" \
  "v_exp_f32 v99, v99\n\t" \
  "v_exp_f32 v100, v100\n\t" \
  "v_smfmac_f32_32x32x32_bf16 %1, v[128:131], v[160:167], %10\n\t" \
  "ds_read_b128 v[128:131], %3 offset:38560\n\t" \
  "v_exp_f32 v101, v101\n\t" \
  "v_exp_f32 v102, v102\n\t" \
  "v_exp_f32 v103, v103\n\t" \
  "v_exp_f32 v104, v104\n\t" \
  "v_exp_f32 v105, v105\n\t" \
  "v_exp_f32 v106, v106\n\t" \
  "v_exp_f32 v107, v107\n\t" \
  "v_exp_f32 v108, v108\n\t" \
  "v_exp_f32 v109, v109\n\t" \
  "v_exp_f32 v110, v110\n\t" \
  "v_exp_f32 v111, v111\n\t" \
  "v_mfma_f32_32x32x16_bf16 v[152:167], v[112:115], %6, %8\n\t" \
  "v_exp_f32 v136, v136\n\t" \
  "v_exp_f32 v137, v137\n\t" \
  "v_exp_f32 v138, v138\n\t" \
  "v_exp_f32 v139, v139\n\t" \
  "v_smfmac_f32_32x32x32_bf16 %0, v[124:127], v[96:103], %10\n\t" \
  "v_exp_f32 v140, v140\n\t" \
  "v_exp_f32 v141, v141\n\t" \
  "v_smfmac_f32_32x32x32_bf16 %0, v[132:135], v[104:111], %10\n\t" \
  "s_waitcnt lgkmcnt(0)\n\t" \
  "v_exp_f32 v142, v142\n\t" \
  "v_exp_f32 v143, v143\n\t" \
  "v_exp_f32 v144, v144\n\t" \
  "v_exp_f32 v145, v145\n\t" \
  "v_exp_f32 v146, v146\n\t" \
  "v_exp_f32 v147, v147\n\t" \
  "v_exp_f32 v148, v148\n\t" \
  "v_exp_f32 v149, v149\n\t" \
  "v_exp_f32 v150, v150\n\t" \
  "v_exp_f32 v151, v151\n\t" \
  "v_mfma_f32_32x32x16_bf16 v[96:111], v[112:115], %7, %9\n\t" \
  "v_exp_f32 v152, v152\n\t" \
  "v_exp_f32 v153, v153\n\t" \
  "v_exp_f32 v154, v154\n\t" \
  "v_smfmac_f32_32x32x32_bf16 %1, v[124:127], v[136:143], %10\n\t" \
  "ds_read_b128 v[124:127], %3 offset:40128\n\t" \
  "v_exp_f32 v155, v155\n\t" \
  "v_exp_f32 v156, v156\n\t" \
  "v_smfmac_f32_32x32x32_bf16 %1, v[132:135], v[144:151], %10\n\t" \
  "ds_read_b128 v[132:135], %3 offset:40672\n\t" \
  "v_exp_f32 v157, v157\n\t" \
  "v_exp_f32 v158, v158\n\t" \
  "v_exp_f32 v159, v159\n\t" \
  "v_exp_f32 v160, v160\n\t" \
  "v_exp_f32 v161, v161\n\t" \
  "v_exp_f32 v162, v162\n\t" \
  "v_exp_f32 v163, v163\n\t" \
  "v_exp_f32 v164, v164\n\t" \
  "v_exp_f32 v165, v165\n\t" \
  "v_exp_f32 v166, v166\n\t" \
  "v_exp_f32 v167, v167\n\t" \
  "v_mfma_f32_32x32x16_bf16 v[136:151], v[116:119], %6, %8\n\t" \
  "v_exp_f32 v96, v96\n\t" \
  "v_exp_f32 v97, v97\n\t" \
  "v_exp_f32 v98, v98\n\t" \
  "v_exp_f32 v99, v99\n\t" \
  "v_smfmac_f32_32x32x32_bf16 %0, v[120:123], v[152:159], %10\n\t" \
  "v_exp_f32 v100, v100\n\t" \
  "v_exp_f32 v101, v101\n\t" \
  "v_smfmac_f32_32x32x32_bf16 %0, v[128:131], v[160:167], %10\n\t" \
  "s_waitcnt lgkmcnt(0)\n\t" \
  "v_exp_f32 v102, v102\n\t" \
  "v_exp_f32 v103, v103\n\t" \
  "v_exp_f32 v104, v104\n\t" \
  "v_exp_f32 v105, v105\n\t" \
  "v_exp_f32 v106, v106\n\t" \
  "v_exp_f32 v107, v107\n\t" \
  "v_exp_f32 v108, v108\n\t" \
  "v_exp_f32 v109, v109\n\t" \
  "v_exp_f32 v110, v110\n\t" \
  "v_exp_f32 v111, v111\n\t" \
  "v_mfma_f32_32x32x16_bf16 v[152:167], v[116:119], %7, %9\n\t" \
  "v_exp_f32 v136, v136\n\t" \
  "v_exp_f32 v137, v137\n\t" \
  "v_exp_f32 v138, v138\n\t" \
  "v_smfmac_f32_32x32x32_bf16 %1, v[120:123], v[96:103], %10\n\t" \
  "v_exp_f32 v139, v139\n\t" \
  "v_exp_f32 v140, v140\n\t" \
  "v_smfmac_f32_32x32x32_bf16 %1, v[128:131], v[104:111], %10\n\t" \
  "v_exp_f32 v141, v141\n\t" \
  "v_exp_f32 v142, v142\n\t" \
  "v_exp_f32 v143, v143\n\t" \
  "v_exp_f32 v144, v144\n\t" \
  "v_exp_f32 v145, v145\n\t" \
  "v_exp_f32 v146, v146\n\t" \
  "v_exp_f32 v147, v147\n\t" \
  "v_exp_f32 v148, v148\n\t" \
  "v_exp_f32 v149, v149\n\t" \
  "v_exp_f32 v150, v150\n\t" \
  "v_exp_f32 v151, v151\n\t" \
  "v_exp_f32 v152, v152\n\t" \
  "v_exp_f32 v153, v153\n\t" \
  "v_exp_f32 v154, v154\n\t" \
  "v_exp_f32 v155, v155\n\t" \
  "v_smfmac_f32_32x32x32_bf16 %0, v[124:127], v[136:143], %10\n\t" \
  "v_exp_f32 v156, v156\n\t" \
  "v_exp_f32 v157, v157\n\t" \
  "v_smfmac_f32_32x32x32_bf16 %0, v[132:135], v[144:151], %10\n\t" \
  "s_waitcnt lgkmcnt(0)\n\t" \
  "v_exp_f32 v158, v158\n\t" \
  "v_exp_f32 v159, v159\n\t" \
  "v_exp_f32 v160, v160\n\t" \
  "v_exp_f32 v161, v161\n\t" \
  "v_exp_f32 v162, v162\n\t" \
  "v_exp_f32 v163, v163\n\t" \
  "v_exp_f32 v164, v164\n\t" \
  "v_exp_f32 v165, v165\n\t" \
  "v_exp_f32 v166, v166\n\t" \
  "v_exp_f32 v167, v167\n\t" \
  "s_nop 1\n\t" \
  "v_smfmac_f32_32x32x32_bf16 %1, v[124:127], v[152:159], %10\n\t" \
  "v_smfmac_f32_32x32x32_bf16 %1, v[132:135], v[160:167], %10\n\t" \
  "s_mov_b32 %5, 0\n\t" \
  "s_branch 9f\n\t" \
  "8:\n\t" \
  "s_mov_b32 %5, 1\n\t" \
  "s_branch 9f\n\t" \
  "11:\n\t" \
  "s_sleep 1\n\t" \
  "ds_read_b32 %4, %11 offset:4\n\t" \
  "s_waitcnt lgkmcnt(0)\n\t" \
  "v_readfirstlane_b32 %5, %4\n\t" \
  "s_nop 0\n\t" \
  "s_cmp_lt_u32 %5, 1\n\t" \
  "s_cbranch_scc1 11b\n\t" \
  "s_branch 21b\n\t" \
  "12:\n\t" \
  "s_sleep 1\n\t" \
  "ds_read_b32 %4, %11 offset:8\n\t" \
  "s_waitcnt lgkmcnt(0)\n\t" \
  "v_readfirstlane_b32 %5, %4\n\t" \
  "s_nop 0\n\t" \
  "s_cmp_lt_u32 %5, 4\n\t" \
  "s_cbranch_scc1 12b\n\t" \
  "s_branch 22b\n\t" \
  "13:\n\t" \
  "s_sleep 1\n\t" \
  "ds_read_b32 %4, %11 offset:12\n\t" \
  "s_waitcnt lgkmcnt(0)\n\t" \
  "v_readfirstlane_b32 %5, %4\n\t" \
  "s_nop 0\n\t" \
  "s_cmp_lt_u32 %5, 8\n\t" \
  "s_cbranch_scc1 13b\n\t" \
  "s_branch 23b\n\t" \
  "14:\n\t" \
  "s_sleep 1\n\t" \
  "ds_read_b32 %4, %11 offset:16\n\t" \
  "s_waitcnt lgkmcnt(0)\n\t" \
  "v_readfirstlane_b32 %5, %4\n\t" \
  "s_nop 0\n\t" \
  "s_cmp_lt_u32 %5, 8\n\t" \
  "s_cbranch_scc1 14b\n\t" \
  "s_branch 24b\n\t" \
  "9:\n\t" \
  "s_waitcnt lgkmcnt(0)\n\t" \
  "s_nop 11\n\t" \
  ""
#define ATTN_CLOBBERS "vcc", "v96", "v97", "v98", "v99", "v100", "v101", "v102", "v103", "v104", "v105", "v106", "v107", "v108", "v109", "v110", "v111", "v112", "v113", "v114", "v115", "v116", "v117", "v118", "v119", "v120", "v121", "v122", "v123", "v124", "v125", "v126", "v127", "v128", "v129", "v130", "v131", "v132", "v133", "v134", "v135", "v136", "v137", "v138", "v139", "v140", "v141", "v142", "v143", "v144", "v145", "v146", "v147", "v148", "v149", "v150", "v151", "v152", "v153", "v154", "v155", "v156", "v157", "v158", "v159", "v160", "v161", "v162", "v163", "v164", "v165", "v166", "v167"


__device__ __forceinline__ unsigned cvtpk(float lo, float hi) {
  bf16x2 r; r[0] = (__bf16)lo; r[1] = (__bf16)hi; return __builtin_bit_cast(unsigned, r); }
__device__ __forceinline__ float bflo(unsigned u) { return __uint_as_float(u << 16); }
__device__ __forceinline__ float bfhi(unsigned u) { return __uint_as_float(u & 0xffff0000u); }
__device__ __forceinline__ float dot2(unsigned a, unsigned b, float c) {
  return __builtin_amdgcn_fdot2_f32_bf16(__builtin_bit_cast(bf16x2, a), __builtin_bit_cast(bf16x2, b), c, false); }
__device__ __forceinline__ void dma16(unsigned voff, const void* sbase, unsigned lds) {
  unsigned sv;
  asm volatile("s_mov_b32 %0, m0\n\ts_mov_b32 m0, %3\n\ts_nop 0\n\tglobal_load_lds_dwordx4 %1, %2\n\ts_mov_b32 m0, %0"
               : "=&s"(sv) : "v"(voff), "s"(sbase), "s"(lds) : "memory"); }
__device__ __forceinline__ void dma16c(unsigned voff, const void* sbase, unsigned lds) {
  unsigned sv;
  asm volatile("s_mov_b32 %0, m0\n\ts_mov_b32 m0, %3\n\ts_nop 0\n\tglobal_load_lds_dwordx4 %1, %2 sc1\n\ts_mov_b32 m0, %0"
               : "=&s"(sv) : "v"(voff), "s"(sbase), "s"(lds) : "memory"); }
__device__ __forceinline__ void touch(unsigned voff, const void* sbase, unsigned L0) {
  unsigned sv;
  asm volatile("s_mov_b32 %0, m0\n\ts_mov_b32 m0, %3\n\ts_nop 0\n\tglobal_load_lds_dword %1, %2\n\ts_mov_b32 m0, %0"
               : "=&s"(sv) : "v"(voff), "s"(sbase), "s"(L0 + L_CTRL - 256) : "memory"); }
__device__ __forceinline__ bf16x8 ldsfrag(unsigned a) { return *(const LAS bf16x8*)(uintptr_t)a; }
__device__ __forceinline__ bf16x8 trfrag(unsigned a0, unsigned a1) {
  v4i16 lo = __builtin_amdgcn_ds_read_tr16_b64_v4i16((LAS v4i16*)(uintptr_t)a0);
  v4i16 hi = __builtin_amdgcn_ds_read_tr16_b64_v4i16((LAS v4i16*)(uintptr_t)a1);
  return __builtin_bit_cast(bf16x8, __builtin_shufflevector(lo, hi, 0, 1, 2, 3, 4, 5, 6, 7)); }
__device__ __forceinline__ unsigned lds_ld(unsigned a) { return *(volatile LAS unsigned*)(uintptr_t)a; }
__device__ __forceinline__ void lds_st(unsigned a, unsigned v) { *(volatile LAS unsigned*)(uintptr_t)a = v; }
__device__ __forceinline__ void lds_st128(unsigned a, u32x4 v) { *(LAS u32x4*)(uintptr_t)a = v; }
template <int CTRL> __device__ __forceinline__ float dpp(float v) {
  return __int_as_float(__builtin_amdgcn_update_dpp(0, __float_as_int(v), CTRL, 0xf, 0xf, true)); }
__device__ __forceinline__ float sum8(float v) { v += dpp<0xB1>(v); v += dpp<0x4E>(v); v += dpp<0x141>(v); return v; }
__device__ __forceinline__ float other_half(float v) {
  auto r = __builtin_amdgcn_permlane32_swap(__float_as_uint(v), __float_as_uint(v), false, false);
  return __uint_as_float((threadIdx.x & 32) ? r[0] : r[1]); }
__device__ __forceinline__ u32x4 pack8(const f32x16& a, int o, float sc) {
  return (u32x4){cvtpk(a[o] * sc, a[o + 1] * sc), cvtpk(a[o + 2] * sc, a[o + 3] * sc), cvtpk(a[o + 4] * sc, a[o + 5] * sc), cvtpk(a[o + 6] * sc, a[o + 7] * sc)}; }
__device__ __forceinline__ f32x2 gelu_tanh2(f32x2 v) {
  constexpr float c0 = -2.0f * 0.7978845608028654f * 1.4426950408889634f, c1 = c0 * 0.044715f;
  const f32x2 z = v * __builtin_elementwise_fma(v * v, (f32x2){c1, c1}, (f32x2){c0, c0});
  const f32x2 d = (f32x2){__builtin_amdgcn_exp2f(z[0]), __builtin_amdgcn_exp2f(z[1])} + (f32x2){1.0f, 1.0f};
  return v * (f32x2){__builtin_amdgcn_rcpf(d[0]), __builtin_amdgcn_rcpf(d[1])}; }
template <int SLEEP> __device__ __forceinline__ void poll(const char* p, u64 tag) {
  bool ok = false;
#pragma unroll 1
  for (unsigned s = 0; s < SPIN_CAP; ++s) {
    if (!ok) ok = __hip_atomic_load((GAS u64*)p, RLX_AGENT) == tag;
    if (__all(ok)) return;
    __builtin_amdgcn_s_sleep(SLEEP); } }
__device__ __forceinline__ unsigned lds_add(unsigned a) {
  return __hip_atomic_fetch_add((LAS unsigned*)(uintptr_t)a, 1u, __ATOMIC_RELAXED, __HIP_MEMORY_SCOPE_WORKGROUP); }
__device__ __forceinline__ void lds_max(unsigned a, unsigned v) { asm volatile("ds_max_u32 %0, %1" :: "v"(a), "v"(v) : "memory"); }
__device__ __forceinline__ void arrive(unsigned a) {
  asm volatile("s_waitcnt lgkmcnt(0)\n\ts_mov_b64 exec, 1\n\tds_add_u32 %0, %1\n\ts_mov_b64 exec, -1" :: "v"(a), "v"(1u) : "memory"); }
template <int SLEEP = 0> __device__ __forceinline__ void lds_wait_ge(unsigned a, unsigned n) {
  asm volatile("" : "+v"(a));
  while ((unsigned)__builtin_amdgcn_readfirstlane(lds_ld(a)) < n) { if (SLEEP) __builtin_amdgcn_s_sleep(SLEEP); }
  asm volatile("" ::: "memory"); }
__device__ __forceinline__ unsigned sweep(__amdgpu_buffer_rsrc_t wsr, unsigned off, u64 tag, u32x4 fl) {
#pragma unroll 1
  for (unsigned s = 0; s < SPIN_CAP; ++s) {
    const bool ok = fl[0] == (unsigned)tag && fl[1] == (unsigned)(tag >> 32);
    if (__all(ok)) break;
    __builtin_amdgcn_s_sleep(2); asm volatile("" ::: "memory");
    if (!ok) fl = __builtin_amdgcn_raw_buffer_load_b128(wsr, off, 0, 16); }
  return fl[2]; }
__device__ __forceinline__ void arrive_and_flag(unsigned cnt, unsigned n, char* flag, u64 tag) {
  if ((threadIdx.x & 63) == 0 && lds_add(cnt) == n - 1) __hip_atomic_store((GAS u64*)flag, tag, RLX_AGENT); }
__device__ __forceinline__ float sum_x16(float v) {
  auto r = __builtin_amdgcn_permlane16_swap(__float_as_uint(v), __float_as_uint(v), false, false); return __uint_as_float(r[0]) + __uint_as_float(r[1]); }
__device__ __forceinline__ float sum_x32(float v) {
  auto r = __builtin_amdgcn_permlane32_swap(__float_as_uint(v), __float_as_uint(v), false, false); return __uint_as_float(r[0]) + __uint_as_float(r[1]); }
__device__ __forceinline__ u64 make_tag(u32x2 e) {
  const u64 v = (u64)(unsigned)__builtin_amdgcn_readfirstlane(e[0]) | ((u64)(unsigned)__builtin_amdgcn_readfirstlane(e[1]) << 32);
  return v * 0x9E3779B97F4A7C15ull + 0xD1B54A32D192ED03ull; }
__device__ __forceinline__ u64 lds_tag(unsigned L0) {
  const u32x2 v = *(const LAS u32x2*)(uintptr_t)(L0 + C_TAG);
  return (u64)(unsigned)__builtin_amdgcn_readfirstlane(v[0]) | ((u64)(unsigned)__builtin_amdgcn_readfirstlane(v[1]) << 32); }
__device__ __forceinline__ f32x16 bias16(const u32x2* bb) {
  f32x16 a;
#pragma unroll
  for (int k = 0; k < 4; ++k) { a[4 * k] = bflo(bb[k][0]); a[4 * k + 1] = bfhi(bb[k][0]); a[4 * k + 2] = bflo(bb[k][1]); a[4 * k + 3] = bfhi(bb[k][1]); }
  return a; }
__device__ __forceinline__ void gelu_store(f32x16& a, __amdgpu_buffer_rsrc_t wsr, unsigned off) {
#pragma unroll
  for (int i = 0; i < 16; i += 2) { const f32x2 y = gelu_tanh2((f32x2){a[i], a[i + 1]}); a[i] = y[0]; a[i + 1] = y[1]; }
  __builtin_amdgcn_raw_buffer_store_b128(pack8(a, 0, 1.0f), wsr, off, 0, 16);
  __builtin_amdgcn_raw_buffer_store_b128(pack8(a, 8, 1.0f), wsr, off + 1024, 0, 16); }
__device__ __forceinline__ void st_tile(unsigned a, const f32x16& v) {
#pragma unroll
  for (int k = 0; k < 4; ++k) *(LAS f32x4*)(uintptr_t)(a + k * 1024) = (f32x4){v[4 * k], v[4 * k + 1], v[4 * k + 2], v[4 * k + 3]}; }
__device__ __forceinline__ void add_tile(f32x16& v, unsigned a) {
#pragma unroll
  for (int k = 0; k < 4; ++k) { const f32x4 t = *(const LAS f32x4*)(uintptr_t)(a + k * 1024);
    v[4 * k] += t[0]; v[4 * k + 1] += t[1]; v[4 * k + 2] += t[2]; v[4 * k + 3] += t[3]; } }
#define W1_LD(k) asm volatile("global_load_dwordx4 %0, %1, %2" : "=&v"(sg[(k) % 10]) : "v"(w1off), "s"(wsrc + (size_t)(k) * 16 * 2752) : "memory")
#define F1_NEXT(c) do { \
    asm volatile("s_waitcnt vmcnt(%2)" : "+v"(sg[(2 * (c)) % 10]), "+v"(sg[(2 * (c) + 1) % 10]) : "n"(30 - 2 * (c) < 8 ? 30 - 2 * (c) : 8) : "memory"); \
    lds_st128(wr, sg[(2 * (c)) % 10]); lds_st128(wr + 1024, sg[(2 * (c) + 1) % 10]); \
    _Pragma("unroll") for (int kk = 0; kk < 2; ++kk) { const int k = 2 * (c) + kk; \
      wf[(c) & 1][kk] = trfrag(tb + kk * 1024, tb + kk * 1024 + 256); xf[(c) & 1][kk][0] = ldsfrag(xb[k & 3] + k * 1024); \
      if (NRT == 2) xf[(c) & 1][kk][1] = ldsfrag(xb[k & 3] + 32768 + k * 1024); } \
    asm volatile("" ::: "memory"); \
    if (2 * (c) + 10 < 32) { W1_LD(2 * (c) + 10); W1_LD(2 * (c) + 11); } } while (0)
template <int NRT> __device__ __forceinline__ void ffn_up(f32x16& a0, f32x16& a1, u32x4 (&sg)[10], const unsigned* xb, unsigned win,
                                                          unsigned w1off, const char* wsrc, int lane) {
  const unsigned wr = win + lane * 16, tb = win + (lane >> 5) * 512 + ((lane & 15) >> 2) * 64 + ((lane >> 4) & 1) * 32 + (lane & 3) * 8;
  bf16x8 wf[2][2], xf[2][2][2];
  F1_NEXT(0);
#pragma unroll
  for (int c = 0; c < 16; ++c) {
    if (c + 1 < 16) F1_NEXT(c + 1);
    SBAR();
#pragma unroll
    for (int kk = 0; kk < 2; ++kk) { a0 = MFMA(wf[c & 1][kk], xf[c & 1][kk][0], a0); if (NRT == 2) a1 = MFMA(wf[c & 1][kk], xf[c & 1][kk][1], a1); }
    asm volatile("" : "+v"(a0), "+v"(a1));
    SBAR();
  } }
__device__ __forceinline__ void stage_kv_rest(unsigned L0, const char* kv, int w8, int lane) {
#pragma unroll
  for (int m = 0; m < 7; ++m) { const int s = 37 * KVT + (w8 + 8 * m) * 1024; dma16c(lane * 16, kv + s, L0 + s - 5 * KVT); }
  VMWAIT(3); arrive(L0 + C_PART + 12); }

#define COMMON_LANE_CONSTANTS \
  const int r = lane & 31, h = lane >> 5, qq = (lane & 15) >> 2; \
  const int ft0 = (43 * j) >> 3, nft = ((43 * (j + 1)) >> 3) - ft0; \
  const __amdgpu_buffer_rsrc_t wsr = __builtin_amdgcn_make_buffer_rsrc(ws, 0, WS_END, 0x00020000); \
  const unsigned w1off = (lane >> 2) * 2752 + (lane & 3) * 16;                                   \
  const unsigned w128 = (lane >> 3) * 1024 + (((lane & 7) ^ (((lane >> 4) & 1) << 2)) << 4);      \
  const unsigned tcol = (((((lane >> 4) & 1) * 2 + ((lane & 3) >> 1)) ^ ((qq >> 1) << 2)) << 4) + (lane & 1) * 8; \
  const char* kvs = ws + WS_KV + (bx & 7) * 8 * KVT;                                            \
  (void)r
#define XN_BASES(off) unsigned xb[4]; { int ln = lane; asm volatile("" : "+v"(ln)); \
  _Pragma("unroll") for (int m = 0; m < 4; ++m) xb[m] = L0 + L_XN + (off) + (ln >> 5) * 512 + (((ln & 31) ^ ((2 * m + (ln >> 5)) & 7)) << 4); }

__global__ __launch_bounds__(768, 3) void fused_block(const char* __restrict__ x, const char* __restrict__ wq, const char* __restrict__ wk,
                                                      const char* __restrict__ wv, const char* __restrict__ wo, const char* __restrict__ w1,
                                                      const char* __restrict__ b1, const char* __restrict__ w2, const char* __restrict__ b2,
                                                      char* __restrict__ out, char* ws) {
  extern __shared__ __attribute__((aligned(16))) char smem[];
  const int tid = threadIdx.x, lane = tid & 63, wid = __builtin_amdgcn_readfirstlane(tid >> 6);
  const int bx = blockIdx.x;
  const unsigned L0 = (unsigned)(uintptr_t)smem;
  if (wid == 0) {
    u64 pc; asm volatile("s_getpc_b64 %0" : "=s"(pc));
    touch(min(((bx >> 3) * 16 + (lane & 15)) * 128u, CODE_BYTES), (const char*)pc, L0);
  }
  asm volatile("" :: "s"(x), "s"(wq), "s"(wk), "s"(wv), "s"(wo), "s"(w1), "s"(b1), "s"(w2), "s"(b2), "s"(out), "s"(ws));
  const int g = ((bx & 7) >> 1) * 8 + (bx >> 5), j = (bx & 1) * 4 + ((bx >> 3) & 3);

  if (wid < 8) {
    const int ft = ((43 * j) >> 3) + (wid & 3);
    u32x4 xr[8]; unsigned pf;
    {
      const unsigned xo = (wid * 8 + (lane >> 3)) * 1024 + (lane & 7) * 16, po = ((bx >> 5) * 64 + wid * 8 + (lane & 7)) * 1024;
      asm volatile("" :: "v"(xo), "v"(po));
      BARRIER();
      asm volatile("global_load_dwordx4 %0, %9, %10\n\tglobal_load_dwordx4 %1, %9, %10 offset:128\n\t"
                   "global_load_dwordx4 %2, %9, %10 offset:256\n\tglobal_load_dwordx4 %3, %9, %10 offset:384\n\t"
                   "global_load_dwordx4 %4, %9, %10 offset:512\n\tglobal_load_dwordx4 %5, %9, %10 offset:640\n\t"
                   "global_load_dwordx4 %6, %9, %10 offset:768\n\tglobal_load_dwordx4 %7, %9, %10 offset:896\n\t"
                   "global_load_dword %8, %11, %12"
                   : "=&v"(xr[0]), "=&v"(xr[1]), "=&v"(xr[2]), "=&v"(xr[3]), "=&v"(xr[4]), "=&v"(xr[5]), "=&v"(xr[6]), "=&v"(xr[7]), "=&v"(pf)
                   : "v"(xo), "s"(x + g * 65536), "v"(po), "s"(wq + j * 128) : "memory");
    }
    COMMON_LANE_CONSTANTS;
    asm volatile("s_waitcnt vmcnt(1)" : "+v"(xr[0]), "+v"(xr[1]), "+v"(xr[2]), "+v"(xr[3]), "+v"(xr[4]), "+v"(xr[5]), "+v"(xr[6]), "+v"(xr[7]) :: "memory");
#define WQ_DMA1(i) dma16(w128, wq + (size_t)(wid * 8 + (i)) * 8192 + j * 128, L0 + (wid * 8 + (i)) * 1024)
#define WQ_DMA(i) do { if (wid < 6) WQ_DMA1(i); } while (0)
    {
      WQ_DMA(0); WQ_DMA(1); SBAR();
      float s[4] = {0.f, 0.f, 0.f, 0.f}; f32x2 d[32];
#pragma unroll
      for (int i = 0; i < 32; ++i) { const unsigned w = xr[i >> 2][i & 3]; s[i & 3] = dot2(w, 0x3f803f80u, s[i & 3]); }
      SBAR(); WQ_DMA(2); WQ_DMA(3); SBAR();
      const float nmean = sum8((s[0] + s[1]) + (s[2] + s[3])) * (-1.0f / 512.0f);
      f32x2 vv = {0.f, 0.f};
#pragma unroll
      for (int t = 0; t < 8; ++t) {
        float q[8];
        asm volatile("v_dot2_f32_bf16 %0, %8, %12, %14\n\tv_dot2_f32_bf16 %1, %8, %13, %14\n\tv_dot2_f32_bf16 %2, %9, %12, %14\n\tv_dot2_f32_bf16 %3, %9, %13, %14\n\t"
                     "v_dot2_f32_bf16 %4, %10, %12, %14\n\tv_dot2_f32_bf16 %5, %10, %13, %14\n\tv_dot2_f32_bf16 %6, %11, %12, %14\n\tv_dot2_f32_bf16 %7, %11, %13, %14\n\ts_nop 2"
                     : "=&v"(q[0]), "=&v"(q[1]), "=&v"(q[2]), "=&v"(q[3]), "=&v"(q[4]), "=&v"(q[5]), "=&v"(q[6]), "=&v"(q[7])
                     : "v"(xr[t][0]), "v"(xr[t][1]), "v"(xr[t][2]), "v"(xr[t][3]), "v"(0x00003f80u), "v"(0x3f800000u), "v"(nmean));
#pragma unroll
        for (int i = 0; i < 4; ++i) { d[4 * t + i] = (f32x2){q[2 * i], q[2 * i + 1]}; vv = __builtin_elementwise_fma(d[4 * t + i], d[4 * t + i], vv); }
        if (t == 3) { SBAR(); WQ_DMA(4); WQ_DMA(5); SBAR(); } }
      SBAR(); WQ_DMA(6); WQ_DMA(7); SBAR();
      const float rstd = __builtin_amdgcn_rsqf(sum8(vv[0] + vv[1]) * (1.0f / 512.0f) + 1e-5f);
      const int c = lane & 7, i8 = lane >> 3;
      const unsigned wa = L0 + L_XN + (wid >> 2) * 32768 + (c >> 1) * 1024 + (c & 1) * 512 + ((((wid & 3) << 3) | (i8 ^ c)) << 4);
#pragma unroll
      for (int t = 0; t < 8; ++t)
      { const f32x2 r2 = {rstd, rstd}, y0 = d[4 * t] * r2, y1 = d[4 * t + 1] * r2, y2 = d[4 * t + 2] * r2, y3 = d[4 * t + 3] * r2;
        lds_st128(wa + t * 4096, (u32x4){cvtpk(y0[0], y0[1]), cvtpk(y1[0], y1[1]), cvtpk(y2[0], y2[1]), cvtpk(y3[0], y3[1])}); }
    }
    asm volatile("s_waitcnt vmcnt(0)" : "+v"(pf) :: "memory");
    arrive(L0 + C_XN);
    if (wid >= 6) {
      lds_wait_ge<1>(L0 + C_KVD, 1);
#pragma unroll
      for (int i = 0; i < 8; ++i) WQ_DMA1(i);
      VMWAIT(0); arrive(L0 + C_WQ2);
    }
    if (wid >= 4) {
      int lane = tid & 63; asm volatile("" : "+v"(lane));
      const int h = lane >> 5;
      const unsigned w1off = (lane >> 2) * 2752 + (lane & 3) * 16;
      const char* wsrc = w1 + ft * 64;
      XN_BASES(0);
      u32x4 sg[10]; u32x2 bb[4];
#pragma unroll
      for (int k = 0; k < 4; ++k) bb[k] = *(const GAS u32x2*)(b1 + ft * 64 + h * 8 + k * 16);
#pragma unroll
      for (int k = 0; k < 4; ++k) W1_LD(k);
      touch(((bx >> 5) * 64 + lane) * 2752 + min((wid & 3) * 128, nft * 64 - 4), w1 + ft0 * 64, L0);
      f32x16 a0 = bias16(bb), a1 = a0;
      asm volatile("" : "+v"(a0), "+v"(a1));
      const unsigned ho = WS_H + ((2 * g) * 86 + 2 * ft) * 1024 + lane * 16;
      lds_wait_ge<1>(L0 + C_QD, 4);
#pragma unroll
      for (int k = 4; k < 10; ++k) W1_LD(k);
      ffn_up<2>(a0, a1, sg, xb, L0 + (wid - 4) * 2048, w1off, wsrc, lane);
      __builtin_amdgcn_s_setprio(2);
      arrive(L0 + C_F1);
      gelu_store(a0, wsr, ho); gelu_store(a1, wsr, ho + 86 * 1024);
      lds_wait_ge<4>(L0 + C_F1, 8);
      const u64 tag = lds_tag(L0);
      stage_kv_rest(L0, kvs, wid - 4, lane);
      VMWAIT(0); arrive(L0 + C_PART + 16);
      arrive_and_flag(L0 + C_HCNT, 8, ws + WS_HFLAG + (g * 8 + j) * 128, tag);
      if (bx == 0 && wid == 4) {
#pragma unroll 1
        for (int i = 0; i < 4; ++i) poll<32>(ws + WS_HFLAG + (i * 64 + lane) * 128, tag);
        if (lane == 0) __hip_atomic_store((GAS u64*)(ws + WS_EPOCH), tag, RLX_AGENT);
      }
      __builtin_amdgcn_endpgm();
    }
    __builtin_amdgcn_s_setprio(2);
    unsigned ka0 = L0 + L_KV0 + lane * 16, va0 = L0 + L_KV0 + 1024 + ((r < 16) ? h * 256 + r * 16 : ((r == 16 || r == 28) ? 512 : 528));
    unsigned tb = L0 + (8 * h + qq) * 128 + (tcol ^ ((wid >> 1) << 6));
    XN_BASES((wid & 1) * 32768);
    f32x16 cA, cB, oA = {}, oB = {};
    asm volatile("" : "+v"(ka0), "+v"(va0), "+v"(tb), "+v"(xb[0]), "+v"(xb[1]), "+v"(xb[2]), "+v"(xb[3]), "+v"(oA), "+v"(oB));
    lds_wait_ge(L0 + C_XN, 8);
    u32x4 qA, qB; float mA = 0.f, mB = 0.f;
    {
      f32x16 d = {};
      bf16x8 wf[6], xf[6];
#define Q_RD(ks) do { wf[(ks) % 6] = trfrag(tb + (ks) * 2048, tb + (ks) * 2048 + 512); xf[(ks) % 6] = ldsfrag(xb[(ks) & 3] + (ks) * 1024); } while (0)
#pragma unroll
      for (int ks = 0; ks < 5; ++ks) Q_RD(ks);
#pragma unroll
      for (int ks = 0; ks < 32; ++ks) {
        if (ks == 19) lds_wait_ge(L0 + C_WQ2, 2);
        if (ks + 5 < 32) Q_RD(ks + 5);
        if (ks == 29) arrive(L0 + C_QD);
        SBAR();
        d = MFMA(wf[ks % 6], xf[ks % 6], d);
        asm volatile("" : "+v"(d)); SBAR(); }
#pragma unroll
      for (int i = 0; i < 16; ++i) d[i] *= QSCALE;
      qA = pack8(d, 0, 1.0f); qB = pack8(d, 8, 1.0f);
#pragma unroll
      for (int i = 0; i < 8; ++i) { mA = fmaf(d[i], d[i], mA); mB = fmaf(d[8 + i], d[8 + i], mB); }
    }
    mA = sum_x32(mA); mB = sum_x32(mB);
    asm volatile("" : "+v"(mA), "+v"(mB), "+v"(qA), "+v"(qB));
    unsigned lim = __float_as_uint(SAFE_BOUND * SAFE_BOUND * 0.98f * __builtin_amdgcn_rcpf(fmaxf(mA, mB)));
    asm volatile("" : "+v"(lim));
    lds_wait_ge(L0 + C_KNR, 3);
    const float kn0 = __uint_as_float(lds_ld(L0 + C_KN0));
    mA = -__builtin_amdgcn_sqrtf(mA * kn0); mB = -__builtin_amdgcn_sqrtf(mB * kn0);
#pragma unroll
    for (int i = 0; i < 16; ++i) { cA[i] = mA; cB[i] = mB; }
    unsigned redo = __any(__float_as_uint(kn0) > lim), idx = 0xddddddddu, ctl = L0 + C_PART;
    asm volatile("" : "+v"(cA), "+v"(cB), "+v"(idx), "+v"(ctl));
    lds_wait_ge(L0 + C_PART, 3);
    __builtin_amdgcn_s_setprio(0);
    for (;;) {
      if (redo) {
        lds_wait_ge<2>(L0 + C_CF, 1);
        mA = -INFINITY; mB = -INFINITY; lim = 0xffffffffu;
        for (int t = 0; t < 64; ++t) {
          const bf16x8 kf = __builtin_bit_cast(bf16x8, __builtin_amdgcn_raw_buffer_load_b128(wsr, WS_KV + t * KVT + lane * 16, 0, 16)); const f32x16 z = {};
          const f32x16 s0 = MFMA(kf, __builtin_bit_cast(bf16x8, qA), z), s1 = MFMA(kf, __builtin_bit_cast(bf16x8, qB), z);
#pragma unroll
          for (int i = 0; i < 16; ++i) { mA = fmaxf(mA, s0[i]); mB = fmaxf(mB, s1[i]); } }
        mA = fmaxf(mA, other_half(mA)); mB = fmaxf(mB, other_half(mB));
        oA = (f32x16){}; oB = (f32x16){};
#pragma unroll
        for (int i = 0; i < 16; ++i) { cA[i] = -mA; cB[i] = -mB; }
      }
      unsigned tmp, ka = ka0, va = va0;
      asm volatile(ATTN_LOOP_ASM
                   : "+v"(oA), "+v"(oB), "+v"(ka), "+v"(va), "=&v"(tmp), "=&s"(redo)
                   : "v"(qA), "v"(qB), "v"(cA), "v"(cB), "v"(idx), "v"(ctl), "v"(lim) : "memory", "scc", ATTN_CLOBBERS);
      if (!redo) break;
    }
    const float iA = __builtin_amdgcn_rcpf(h ? oA[12] : oA[8]), iB = __builtin_amdgcn_rcpf(h ? oB[12] : oB[8]);
    const unsigned oo = WS_O + ((2 * g + (wid & 1)) * 32 + 4 * j + (wid >> 1) * 2) * 1024 + lane * 16;
    __builtin_amdgcn_raw_buffer_store_b128(pack8(oA, 0, iA), wsr, oo, 0, 16);
    __builtin_amdgcn_raw_buffer_store_b128(pack8(oB, 0, iB), wsr, oo + 1024, 0, 16);
    const u64 tag = lds_tag(L0);
    VMWAIT(0);
    arrive_and_flag(L0 + C_OCNT, 4, ws + WS_OFLAG + (g * 8 + j) * 128, tag);
    __builtin_amdgcn_endpgm();
  }

  const int u = wid - 8, c8 = lane & 7, kg = lane >> 4;
  __builtin_amdgcn_s_setprio(3);
  u32x4 xr[4]; u32x2 ep; unsigned pv;
  const unsigned wlk = L0 + L_WK + u * 4096, wlv = L0 + L_WV + u * 4096;
  {
    const unsigned xo = c8 * 1024, xo2 = xo + u * 256 + kg * 16, zo = 0;
    asm volatile("global_load_dwordx4 %0, %6, %8\n\tglobal_load_dwordx4 %1, %6, %8 offset:64\n\t"
                 "global_load_dwordx4 %2, %6, %8 offset:128\n\tglobal_load_dwordx4 %3, %6, %8 offset:192\n\t"
                 "global_load_ushort %4, %7, %8\n\tglobal_load_dwordx2 %5, %9, %10 sc1"
                 : "=&v"(xr[0]), "=&v"(xr[1]), "=&v"(xr[2]), "=&v"(xr[3]), "=&v"(pv), "=&v"(ep)
                 : "v"(xo2), "v"(xo), "s"(x + (g * 64 + j * 8) * 1024), "v"(zo), "s"(u == 3 ? ws + WS_EPOCH : x + (g * 64 + j * 8) * 1024) : "memory");
    if (u == 3 && lane < 32) lds_st(L0 + L_CTRL + lane * 4, 0u);
  }
  BARRIER();
  {
    __builtin_amdgcn_s_sleep(8);
    unsigned sv;
    asm volatile("s_mov_b32 %0, m0\n\ts_mov_b32 m0, %4\n\ts_nop 0\n\t"
                 "global_load_lds_dwordx4 %1, %2\n\tglobal_load_lds_dwordx4 %1, %2 offset:1024\n\t"
                 "global_load_lds_dwordx4 %1, %2 offset:2048\n\tglobal_load_lds_dwordx4 %1, %2 offset:3072\n\t"
                 "s_mov_b32 m0, %5\n\ts_nop 0\n\t"
                 "global_load_lds_dwordx4 %1, %3\n\tglobal_load_lds_dwordx4 %1, %3 offset:1024\n\t"
                 "global_load_lds_dwordx4 %1, %3 offset:2048\n\tglobal_load_lds_dwordx4 %1, %3 offset:3072\n\ts_mov_b32 m0, %0"
                 : "=&s"(sv) : "v"(lane * 16), "s"(wk + u * 4096), "s"(wv + u * 4096), "s"(wlk), "s"(wlv) : "memory");
  }
  COMMON_LANE_CONSTANTS;
  u64 tag;
  {
    asm volatile("s_waitcnt vmcnt(8)" : "+v"(xr[0]), "+v"(xr[1]), "+v"(xr[2]), "+v"(xr[3]), "+v"(pv), "+v"(ep) :: "memory");
    if (u == 3 && lane == 0) *(LAS u64*)(uintptr_t)(L0 + C_TAG) = make_tag(ep);
    float d[32], s0 = 0.f, s1 = 0.f, q0 = 0.f, q1 = 0.f; const float piv = bflo(pv);
#pragma unroll
    for (int i = 0; i < 16; ++i) { const unsigned w = xr[i >> 2][i & 3]; d[2 * i] = bflo(w) - piv; d[2 * i + 1] = bfhi(w) - piv;
      s0 += d[2 * i]; s1 += d[2 * i + 1]; q0 = fmaf(d[2 * i], d[2 * i], q0); q1 = fmaf(d[2 * i + 1], d[2 * i + 1], q1); }
    s0 = sum_x32(sum_x16(s0 + s1)); q0 = sum_x32(sum_x16(q0 + q1));
    if (lane < 8) *(LAS u32x2*)(uintptr_t)(L0 + L_KVS + lane * 32 + u * 8) = (u32x2){__float_as_uint(s0), __float_as_uint(q0)};
    arrive(L0 + C_KVS1);
    VMWAIT(0);
    const unsigned to = (8 * kg + qq) * 32 + (lane & 3) * 8, tk = wlk + to, tv = wlv + to;
    bf16x8 fk[4], fv[4];
#pragma unroll
    for (int s = 0; s < 4; ++s) { fk[s] = trfrag(tk + s * 1024, tk + s * 1024 + 128); fv[s] = trfrag(tv + s * 1024, tv + s * 1024 + 128); }
    lds_wait_ge(L0 + C_KVS1, 4);
    tag = lds_tag(L0);
    const f32x4 sa = *(const LAS f32x4*)(uintptr_t)(L0 + L_KVS + c8 * 32), sb = *(const LAS f32x4*)(uintptr_t)(L0 + L_KVS + c8 * 32 + 16);
    const float mean = ((sa[0] + sa[2]) + (sb[0] + sb[2])) * (1.0f / 512.0f);
    const float var = fmaxf(((sa[1] + sa[3]) + (sb[1] + sb[3])) * (1.0f / 512.0f) - mean * mean, 0.f);
    const float rstd = __builtin_amdgcn_rsqf(var + 1e-5f), nb = -mean * rstd;
    f32x4 dk = {}, dv = {};
#pragma unroll
    for (int s = 0; s < 4; ++s) {
      const u32x4 xw = {cvtpk(fmaf(d[8 * s], rstd, nb), fmaf(d[8 * s + 1], rstd, nb)), cvtpk(fmaf(d[8 * s + 2], rstd, nb), fmaf(d[8 * s + 3], rstd, nb)),
                        cvtpk(fmaf(d[8 * s + 4], rstd, nb), fmaf(d[8 * s + 5], rstd, nb)), cvtpk(fmaf(d[8 * s + 6], rstd, nb), fmaf(d[8 * s + 7], rstd, nb))};
      const bf16x8 xf = __builtin_bit_cast(bf16x8, xw);
      dk = __builtin_amdgcn_mfma_f32_16x16x32_bf16(fk[s], xf, dk, 0, 0, 0);
      dv = __builtin_amdgcn_mfma_f32_16x16x32_bf16(xf, fv[s], dv, 0, 0, 0); }
    if (u != 0) { *(LAS f32x4*)(uintptr_t)(L0 + L_KVP + u * 2048 + lane * 16) = dk; *(LAS f32x4*)(uintptr_t)(L0 + L_KVP + u * 2048 + 1024 + lane * 16) = dv; }
    arrive(L0 + C_KVS2);
    if (u == 0) {
      lds_wait_ge(L0 + C_KVS2, 4);
#pragma unroll
      for (int p = 1; p < 4; ++p) { dk += *(const LAS f32x4*)(uintptr_t)(L0 + L_KVP + p * 2048 + lane * 16); dv += *(const LAS f32x4*)(uintptr_t)(L0 + L_KVP + p * 2048 + 1024 + lane * 16); }
      asm volatile("" : "+v"(dk), "+v"(dv));
      arrive(L0 + C_KVD);
      const unsigned T = WS_KV + ((bx & 7) * 8 + (g & 7)) * KVT, VB = T + 1024 + ((j >> 1) & 1) * VST;
      const u32x2 kw = {cvtpk(dk[0], dk[1]), cvtpk(dk[2], dk[3])};
      if ((g & 7) < 6) {
        const unsigned G = WS_G + ((bx & 7) * 6 + (g & 7)) * 4096, t32 = (unsigned)tag;
        if ((lane & 15) < 8) __builtin_amdgcn_raw_buffer_store_b128((u32x4){kw[0], t32, kw[1], t32}, wsr, G + ((kg & 1) * 32 + 8 * (j & 3) + c8) * 32 + (kg >> 1) * 16, 0, 16);
        if (lane < 32) __builtin_amdgcn_raw_buffer_store_b128((u32x4){cvtpk(dv[0], dv[1]), t32, cvtpk(dv[2], dv[3]), t32}, wsr, G + 2048 + (j & 3) * 512 + (lane & 15) * 32 + kg * 16, 0, 16);
      }
      arrive(L0 + C_KVF);
      if ((lane & 15) < 8) { const unsigned a = T + ((kg & 1) * 32 + 8 * (j & 3) + c8) * 16 + (kg >> 1) * 8;
        __builtin_amdgcn_raw_buffer_store_b64(kw, wsr, a, 0, 16); __builtin_amdgcn_raw_buffer_store_b64(kw, wsr, a, KVB, 16); }
      const unsigned cw = lane < 34 ? 0x3f803f80u : 0u;
      const u32x2 vw = lane < 32 ? (u32x2){cvtpk(dv[0], dv[1]), cvtpk(dv[2], dv[3])} : (u32x2){cw, cw};
      if (lane < ((j & 1) ? 32 : 36)) { const unsigned a = lane < 32 ? VB + (j & 1) * 256 + (lane & 15) * 16 + kg * 8 : VB + 256 + lane * 8;
        __builtin_amdgcn_raw_buffer_store_b64(vw, wsr, a, 0, 16); __builtin_amdgcn_raw_buffer_store_b64(vw, wsr, a, KVB, 16); }
      float n2 = bflo(kw[0]) * bflo(kw[0]) + bfhi(kw[0]) * bfhi(kw[0]) + bflo(kw[1]) * bflo(kw[1]) + bfhi(kw[1]) * bfhi(kw[1]);
      n2 = sum_x32(sum_x16(n2)); n2 = fmaxf(n2, dpp<0xB1>(n2)); n2 = fmaxf(n2, dpp<0x4E>(n2)); n2 = fmaxf(n2, dpp<0x141>(n2));
      char* kflag = ws + WS_KVFLAG + ((bx & 7) * 32 + (bx >> 3)) * 128;
      if (lane == 0) __hip_atomic_store((GAS unsigned*)(kflag + 8), __float_as_uint(n2), RLX_AGENT);
      VMWAIT(0);
      if (lane == 0) __hip_atomic_store((GAS u64*)kflag, tag, RLX_AGENT);
    }
  }
  f32x16 ini;
  {
    const char* xp = x + (size_t)(64 * g + 32 * (u & 1) + r) * 1024 + (64 * j + 32 * (u >> 1) + 4 * h) * 2;
    const char* bp = b2 + (64 * j + 32 * (u >> 1) + 4 * h) * 2;
#pragma unroll
    for (int k = 0; k < 4; ++k) { const u32x2 a = *(const GAS u32x2*)(xp + k * 16), b = *(const GAS u32x2*)(bp + k * 16);
      ini[4 * k] = bflo(a[0]) + bflo(b[0]); ini[4 * k + 1] = bfhi(a[0]) + bfhi(b[0]); ini[4 * k + 2] = bflo(a[1]) + bflo(b[1]); ini[4 * k + 3] = bfhi(a[1]) + bfhi(b[1]); }
  }
  {
    const int ftc = ft0 + 4 + (u >> 1);
    const bool hasf = u < 2 || nft == 6;
    const char* wsrc = w1 + (hasf ? ftc : ft0) * 64;
    u32x2 bb[4];
    XN_BASES((u & 1) * 32768);
#pragma unroll
    for (int k = 0; k < 4; ++k) bb[k] = *(const GAS u32x2*)(b1 + (hasf ? ftc : 0) * 64 + h * 8 + k * 16);
    f32x16 a0 = bias16(bb), a1 = {};
    asm volatile("" : "+v"(ini), "+v"(a0), "+v"(xb[0]), "+v"(xb[1]), "+v"(xb[2]), "+v"(xb[3]));
    if (u != 0) {
      const unsigned kt = L0 + L_KV0 + (u - 1) * 2 * KVT, go = WS_G + ((bx & 7) * 6 + (u - 1) * 2) * 4096 + lane * 16, t32 = (unsigned)tag;
      lds_wait_ge<1>(L0 + C_KVF, 1);
      if (lane < 8) { const unsigned cw = (lane & 1) ? 0u : 0x3f803f80u;
        lds_st128(kt + (lane >> 2) * KVT + 1024 + ((lane >> 1) & 1) * VST + 512 + (lane & 1) * 16, (u32x4){cw, cw, cw, cw}); }
      __builtin_amdgcn_s_sleep(12);
      u32x4 gr[8];
#pragma unroll 1
      for (unsigned s = 0; s < SPIN_CAP; ++s) {
        bool ok = true;
#pragma unroll
        for (int i = 0; i < 8; ++i) gr[i] = __builtin_amdgcn_raw_buffer_load_b128(wsr, go + i * 1024, 0, 16);
#pragma unroll
        for (int i = 0; i < 8; ++i) ok = ok && gr[i][1] == t32 && gr[i][3] == t32;
        if (__all(ok)) break;
        __builtin_amdgcn_s_sleep(2); asm volatile("" ::: "memory"); }
      float n2 = 0.f;
#pragma unroll
      for (int t = 0; t < 2; ++t) { float s = 0.f;
#pragma unroll
        for (int i = 0; i < 4; ++i) { const unsigned w = gr[4 * t + (i >> 1)][(i & 1) * 2]; s = fmaf(bflo(w), bflo(w), s); s = fmaf(bfhi(w), bfhi(w), s); }
        n2 = fmaxf(n2, s + dpp<0xB1>(s)); }
      lds_max(L0 + C_KN0, __float_as_uint(n2)); arrive(L0 + C_KNR);
#pragma unroll
      for (int i = 0; i < 8; ++i) { const unsigned a = kt + lane * 8 + (i >> 2) * KVT + ((i & 3) < 2 ? (i & 3) * 512 : 1024 + ((i & 3) - 2) * VST);
        *(LAS u32x2*)(uintptr_t)a = (u32x2){gr[i][0], gr[i][2]}; }
      arrive(L0 + C_PART);
      lds_wait_ge<2>(L0 + C_CF, 1);
    } else {
      __builtin_amdgcn_s_sleep(16);
      u32x4 fl[4];
#pragma unroll
      for (int i = 0; i < 4; ++i) fl[i] = __builtin_amdgcn_raw_buffer_load_b128(wsr, WS_KVFLAG + (i * 64 + lane) * 128, 0, 16);
#pragma unroll
      for (int i = 0; i < 4; ++i) lds_max(L0 + C_KNA, sweep(wsr, WS_KVFLAG + (i * 64 + lane) * 128, tag, fl[i]));
      arrive(L0 + C_CF);
#pragma unroll
      for (int p = 12; p < 27; ++p) dma16c(lane * 16, kvs + p * 1024, L0 + L_KV0 + p * 1024);
      VMWAIT(0); arrive(L0 + C_PART + 4);
    }
    lds_wait_ge<1>(L0 + C_QD, 4);
#pragma unroll
    for (int i = 0; i < 13; ++i) { const int s = 13 * KVT - 512 + min(u + 4 * i, 49) * 1024; dma16c(lane * 16, kvs + s, L0 + s - 5 * KVT); }
    u32x4 sg[10];
#pragma unroll
    for (int k = 0; k < 10; ++k) W1_LD(k);
    VMWAIT(10); arrive(L0 + C_PART + 8);
    if (hasf) {
      __builtin_amdgcn_s_setprio(0);
      ffn_up<1>(a0, a1, sg, xb, L0 + (4 + u) * 2048, w1off, wsrc, lane);
    } else {
      asm volatile("s_waitcnt vmcnt(0)" : "+v"(sg[0]), "+v"(sg[1]), "+v"(sg[2]), "+v"(sg[3]), "+v"(sg[4]), "+v"(sg[5]), "+v"(sg[6]), "+v"(sg[7]),
                   "+v"(sg[8]), "+v"(sg[9]) :: "memory");
    }
    __builtin_amdgcn_s_setprio(2);
    arrive(L0 + C_F1);
    touch((u < 3 ? min((bx >> 5) * 172 + u * 64 + lane, 1375) : (bx >> 5) * 64 + lane) * 1024u, (u < 3 ? w2 : wo) + j * 128, L0);
    if (hasf) gelu_store(a0, wsr, WS_H + ((2 * g + (u & 1)) * 86 + 2 * ftc) * 1024 + lane * 16);
  }
  lds_wait_ge<4>(L0 + C_F1, 8);
  stage_kv_rest(L0, kvs, 4 + u, lane);
  VMWAIT(0); arrive(L0 + C_PART + 16);
  arrive_and_flag(L0 + C_HCNT, 8, ws + WS_HFLAG + (g * 8 + j) * 128, tag);
  lds_wait_ge<8>(L0 + C_MID + 8, 4);
  f32x16 acc[4] = {ini, {}, {}, {}};
  const unsigned tb0 = ((4 * h + qq) * 128 + tcol) ^ ((u >> 1) << 6), tb1 = tb0 ^ 64;
  {
    const int ks0 = (86 * u + 2) >> 2, n = ((86 * (u + 1) + 2) >> 2) - ks0;
    const unsigned ring = L0 + L_W2 + u * 6144;
    const char* w2s = w2 + (size_t)ks0 * 16384 + j * 128;
    const char* hs = ws + WS_H + (size_t)((2 * g + (u & 1)) * 86 + ks0) * 1024;
    const char* ht = ws + WS_H + (size_t)((2 * g + ((u & 1) ^ 1)) * 86 + ks0) * 1024;
#define W2_DMA(k, slot) do { dma16(w128, w2s + (size_t)(k) * 16384, ring + (slot) * 2048); dma16(w128, w2s + (size_t)(k) * 16384 + 8192, ring + (slot) * 2048 + 1024); } while (0)
#define H_LD(k, s) asm volatile("global_load_dwordx4 %0, %2, %3 sc1\n\tglobal_load_dwordx4 %1, %2, %4 sc1" : "=&v"(hf[s][0]), "=&v"(hf[s][1]) \
                                : "v"(lane * 16), "s"(hs + (size_t)(k) * 1024), "s"(ht + (size_t)(k) * 1024) : "memory")
    u32x4 hf[3][2];
    W2_DMA(0, 0); W2_DMA(1, 1); W2_DMA(2, 2);
    poll<16>(ws + WS_HFLAG + (g * 8 + min(2 * u + min(lane, 2), 7)) * 128, tag);
    H_LD(0, 0); H_LD(1, 1); H_LD(2, 2);
    __builtin_amdgcn_s_setprio(0);
#pragma unroll
    for (int i = 0; i < 22; ++i) {
      const int s = i % 3;
      if (i == 0) asm volatile("s_waitcnt vmcnt(4)" : "+v"(hf[s][0]), "+v"(hf[s][1]) :: "memory");
      else if (i == 1) asm volatile("s_waitcnt vmcnt(6)" : "+v"(hf[s][0]), "+v"(hf[s][1]) :: "memory");
      else if (i <= 19) asm volatile("s_waitcnt vmcnt(8)" : "+v"(hf[s][0]), "+v"(hf[s][1]) :: "memory");
      else if (i == 20) asm volatile("s_waitcnt vmcnt(4)" : "+v"(hf[s][0]), "+v"(hf[s][1]) :: "memory");
      else asm volatile("s_waitcnt vmcnt(0)" : "+v"(hf[s][0]), "+v"(hf[s][1]) :: "memory");
      const unsigned sl = ring + s * 2048;
      bf16x8 f0 = trfrag(sl + tb0, sl + tb0 + 1024), f1 = trfrag(sl + tb1, sl + tb1 + 1024);
      asm volatile("s_waitcnt lgkmcnt(0)" : "+v"(f0), "+v"(f1) :: "memory");
      if (i + 3 < 22) { const int kn = (i + 3 == 21) ? n - 1 : i + 3; W2_DMA(kn, s); }
      if (i < 21 || n == 22) {
        const bf16x8 h0 = __builtin_bit_cast(bf16x8, hf[s][0]), h1 = __builtin_bit_cast(bf16x8, hf[s][1]);
        acc[0] = MFMA(f0, h0, acc[0]); acc[1] = MFMA(f0, h1, acc[1]); acc[2] = MFMA(f1, h0, acc[2]); acc[3] = MFMA(f1, h1, acc[3]);
      }
      asm volatile("" : "+v"(acc[0]), "+v"(acc[1]), "+v"(acc[2]), "+v"(acc[3]));
      if (i + 3 < 22) { const int kn = (i + 3 == 21) ? n - 1 : i + 3; H_LD(kn, s); }
    }
  }
  __builtin_amdgcn_s_setprio(2);
  f32x16 e0 = acc[0], e1 = acc[2];
  st_tile(L0 + u * 4096 + lane * 16, acc[1]); st_tile(L0 + L_W2 + u * 6144 + lane * 16, acc[3]);
  arrive(L0 + C_PR + (u >> 1) * 4); lds_wait_ge<2>(L0 + C_PR + (u >> 1) * 4, 2);
  add_tile(e0, L0 + (u ^ 1) * 4096 + lane * 16); add_tile(e1, L0 + L_W2 + (u ^ 1) * 6144 + lane * 16);
  asm volatile("" : "+v"(e0), "+v"(e1));
  lds_wait_ge<32>(L0 + C_MID + 16, 4);
  {
    const char* wos = wo + (size_t)u * 131072 + j * 128;
#pragma unroll
    for (int p = 0; p < 16; ++p) dma16(w128, wos + (size_t)p * 8192, L0 + L_WO + u * 16384 + p * 1024);
    VMWAIT(0); arrive(L0 + C_WO + (u >> 1) * 4);
    __builtin_amdgcn_s_sleep(16);
    lds_wait_ge<2>(L0 + C_WO + (u >> 1) * 4, 2);
    unsigned w0 = L0 + L_WO + (u >> 1) * 32768 + tb0, w1b = L0 + L_WO + (u >> 1) * 32768 + tb1;
    asm volatile("" : "+v"(w0), "+v"(w1b));
    bf16x8 f0[2], f1[2];
#define WO_RD(k) do { f0[(k) & 1] = trfrag(w0 + (k) * 2048, w0 + (k) * 2048 + 1024); f1[(k) & 1] = trfrag(w1b + (k) * 2048, w1b + (k) * 2048 + 1024); } while (0)
    WO_RD(0);
    lds_wait_ge<2>(L0 + C_OCNT, 4);
    poll<1>(ws + WS_OFLAG + (g * 8 + (u >> 1) * 4 + (lane & 3)) * 128, tag);
    const char* os = ws + WS_O + (size_t)((2 * g + (u & 1)) * 32 + 16 * (u >> 1)) * 1024;
    u32x4 of[16];
#define O_LD(a) asm volatile("global_load_dwordx4 %0, %4, %5 offset:-2048 sc1\n\tglobal_load_dwordx4 %1, %4, %5 offset:-1024 sc1\n\t" \
                             "global_load_dwordx4 %2, %4, %5 sc1\n\tglobal_load_dwordx4 %3, %4, %5 offset:1024 sc1" \
        : "=&v"(of[a]), "=&v"(of[a + 1]), "=&v"(of[a + 2]), "=&v"(of[a + 3]) : "v"(lane * 16), "s"(os + (a) * 1024 + 2048) : "memory")
    O_LD(0); O_LD(4); O_LD(8); O_LD(12);
#pragma unroll
    for (int k = 0; k < 16; ++k) {
      if (k + 1 < 16) WO_RD(k + 1);
      asm volatile("s_waitcnt vmcnt(%1)" : "+v"(of[k]) : "n"(15 - k) : "memory");
      const bf16x8 ob = __builtin_bit_cast(bf16x8, of[k]);
      e0 = MFMA(f0[k & 1], ob, e0); e1 = MFMA(f1[k & 1], ob, e1);
    }
  }
  f32x16 own = e0;
  st_tile(L0 + L_RED + u * 4096 + lane * 16, e1);
  BARRIER();
  add_tile(own, L0 + L_RED + (u ^ 2) * 4096 + lane * 16);
  char* op = out + (size_t)(64 * g + 32 * (u & 1) + r) * 1024 + (64 * j + 32 * (u >> 1) + 8 * h) * 2;
#pragma unroll
  for (int m = 0; m < 2; ++m) {
    unsigned a0 = cvtpk(own[8 * m], own[8 * m + 1]), a1 = cvtpk(own[8 * m + 2], own[8 * m + 3]), c0 = cvtpk(own[8 * m + 4], own[8 * m + 5]), c1 = cvtpk(own[8 * m + 6], own[8 * m + 7]);
    auto s0 = __builtin_amdgcn_permlane32_swap(a0, c0, false, false); auto s1 = __builtin_amdgcn_permlane32_swap(a1, c1, false, false);
    *(GAS u32x4*)(op + m * 32) = (u32x4){s0[0], s1[0], s0[1], s1[1]};
  }
}

extern "C" void kernel_launch(void* const* d_in, const int* in_sizes, int n_in, void* d_out, int out_size, void* d_ws, size_t ws_size,
                              hipStream_t stream) {
  (void)in_sizes; (void)n_in; (void)out_size; (void)ws_size;
  (void)hipFuncSetAttribute((const void*)fused_block, hipFuncAttributeMaxDynamicSharedMemorySize, L_TOTAL);
  fused_block<<<256, 768, L_TOTAL, stream>>>((const char*)d_in[0], (const char*)d_in[1], (const char*)d_in[2], (const char*)d_in[3], (const char*)d_in[4],
                                             (const char*)d_in[6], (const char*)d_in[7], (const char*)d_in[8], (const char*)d_in[9], (char*)d_out, (char*)d_ws);
}
```
